```python
import torch

def _softmax(x, axis=-1):
    return torch.softmax(x, dim=axis)
def _gelu(x):
    return torch.nn.functional.gelu(x, approximate="tanh")
def _silu(x):
    return torch.nn.functional.silu(x)
def _sigmoid(x):
    return torch.sigmoid(x)
def _layernorm(x, axis=-1, eps=1e-5):
    m = x.mean(dim=axis, keepdim=True)
    v = x.var(dim=axis, keepdim=True, unbiased=False)
    return (x - m) / torch.sqrt(v + eps)
def _scatter_add(tgt, idx, src, axis=0):
    return tgt.clone().index_add_(axis, idx, src)
def _segment_sum(src, seg, n):
    out = torch.zeros((n,) + src.shape[1:], dtype=src.dtype)
    return out.index_add_(0, seg, src)
def _pad(x, pw):
    flat = tuple(v for pair in reversed(pw) for v in pair)
    return torch.nn.functional.pad(x, flat)

kernel_name = 'interleave_layers__spectral_gfnet__dense_activation_gelu__prompt'

def setup_inputs():

    _w = (lambda a: (lambda t: t.to(torch.bfloat16) if t.is_floating_point() else t)(torch.as_tensor(a)))
    def _A_setup():
        B, S, D = 1, 2048, 640
        Df = D // 2 + 1
        return {
            "x": _w((lambda *s: torch.randn(*s, dtype=torch.bfloat16) * 0.05)(B, S, D)),
            "w_r": _w((lambda *s: torch.randn(*s, dtype=torch.bfloat16) * 0.4)(S, Df) * 0.02),
            "w_i": _w((lambda *s: torch.randn(*s, dtype=torch.bfloat16) * 0.4)(S, Df) * 0.02),
        }
    _A_in = _A_setup()

    def _B_setup():
        B, S, D, F = 1, 2048, 640, 1536
        return {
            "x": _w((lambda *s: torch.randn(*s, dtype=torch.bfloat16) * 0.05)(B, S, D)),
            "w1": _w((lambda *s: torch.randn(*s, dtype=torch.bfloat16) * 0.4)(D, F) * (D ** -0.5)),
            "b1": _w((lambda *s: torch.randn(*s, dtype=torch.bfloat16) * 12.8)(F) * 0.01),
            "w2": _w((lambda *s: torch.randn(*s, dtype=torch.bfloat16) * 0.1)(F, D) * (F ** -0.5)),
            "b2": _w((lambda *s: torch.randn(*s, dtype=torch.bfloat16) * 0.8)(D) * 0.01),
        }
    _B_in = _B_setup()

    return {
        "A_x": _A_in["x"],
        "A_w_r": _A_in["w_r"],
        "A_w_i": _A_in["w_i"],
        "B_x": _B_in["x"],
        "B_w1": _B_in["w1"],
        "B_b1": _B_in["b1"],
        "B_w2": _B_in["w2"],
        "B_b2": _B_in["b2"],
    }

def reference(A_x, A_w_r, A_w_i, B_x, B_w1, B_b1, B_w2, B_b2):
    def _A_ref(x, w_r, w_i):
        B, S, D = x.shape
        X = (lambda x, axes=(-2,-1): torch.fft.rfft2(x.float(), dim=axes))(x, axes=(-2, -1))
        W = w_r + 1j * w_i
        X = X * W
        y = (lambda x, s, axes=(-2,-1): torch.fft.irfft2(x, s=s, dim=axes).to(torch.bfloat16))(X, (S, D), axes=(-2, -1))
        return y

    def _B_ref(x, w1, b1, w2, b2):
        h = torch.einsum("bsd,df->bsf", x, w1) + b1
        h = _gelu(h)
        return torch.einsum("bsf,fd->bsd", h, w2) + b2

    x = A_x
    x = x + _A_ref(_layernorm(x, axis=-1), A_w_r, A_w_i)
    x = x + _B_ref(_layernorm(x, axis=-1), B_w1, B_b1, B_w2, B_b2)
    x = x + _A_ref(_layernorm(x, axis=-1), A_w_r, A_w_i)
    x = x + _B_ref(_layernorm(x, axis=-1), B_w1, B_b1, B_w2, B_b2)
    return x
```

```cpp
#include <hip/hip_runtime.h>
#include <stdint.h>

typedef float f32x4 __attribute__((ext_vector_type(4)));
typedef float f32x2 __attribute__((ext_vector_type(2)));
typedef unsigned u32x4 __attribute__((ext_vector_type(4)));
typedef unsigned u32x2 __attribute__((ext_vector_type(2)));
typedef __bf16 bf16x8 __attribute__((ext_vector_type(8)));
typedef __bf16 bf16x2 __attribute__((ext_vector_type(2)));
typedef __amdgpu_buffer_rsrc_t rsrc_t;
#define DI __device__ __forceinline__
#define GAS __attribute__((address_space(1)))
#define LAS __attribute__((address_space(3)))

constexpr int S = 2048, D = 640, F = 1536;
constexpr unsigned XH_BYTES = 160u * 2048u * 8u;
constexpr unsigned O_XH0 = 0, O_XH1 = XH_BYTES, O_XH2 = 2 * XH_BYTES, O_XH3 = 3 * XH_BYTES;
constexpr unsigned O_HF1 = 0x0A00000, O_HF2 = 0x1000000;
constexpr unsigned O_W1F = 0x1600000;
constexpr unsigned O_W2F = 0x1800000;
constexpr unsigned O_W2T = 0x1A00000;
constexpr unsigned O_WFQ = 0x1C00000;
constexpr unsigned O_ST0 = 0x1F00000, O_ST2 = 0x1F10000;
constexpr unsigned O_B2P = 0x1F20000;
constexpr unsigned WS_USED = 0x2000000;

struct Params {
    const uint16_t* x; const uint16_t* wr; const uint16_t* wi; const uint16_t* w1; const uint16_t* b1;
    const uint16_t* w2; const uint16_t* b2; uint16_t* out; char* ws;
};

DI rsrc_t mkrs(const void* p, unsigned bytes) { return __builtin_amdgcn_make_buffer_rsrc((void*)p, 0, bytes, 0x00020000); }
DI u32x4 ld16(rsrc_t r, unsigned off) { return __builtin_amdgcn_raw_buffer_load_b128(r, off, 0, 0); }
DI u32x2 ld8(rsrc_t r, unsigned off) { return __builtin_amdgcn_raw_buffer_load_b64(r, off, 0, 0); }
DI unsigned ld4(rsrc_t r, unsigned off) { return __builtin_amdgcn_raw_buffer_load_b32(r, off, 0, 0); }
DI unsigned ld2(rsrc_t r, unsigned off) { return __builtin_amdgcn_raw_buffer_load_b16(r, off, 0, 0); }
DI void st16(rsrc_t r, unsigned off, u32x4 v) { __builtin_amdgcn_raw_buffer_store_b128(v, r, off, 0, 0); }
DI void st8(rsrc_t r, unsigned off, u32x2 v) { __builtin_amdgcn_raw_buffer_store_b64(v, r, off, 0, 0); }
DI void st4(rsrc_t r, unsigned off, unsigned v) { __builtin_amdgcn_raw_buffer_store_b32(v, r, off, 0, 0); }
DI void st2(rsrc_t r, unsigned off, unsigned v) { __builtin_amdgcn_raw_buffer_store_b16((unsigned short)v, r, off, 0, 0); }
DI f32x4 mfma(u32x4 a, u32x4 b, f32x4 c) {
    return __builtin_amdgcn_mfma_f32_16x16x32_bf16(__builtin_bit_cast(bf16x8, a), __builtin_bit_cast(bf16x8, b), c, 0, 0, 0);
}
DI unsigned pk(float lo, float hi) { bf16x2 v = __builtin_convertvector((f32x2){lo, hi}, bf16x2); return __builtin_bit_cast(unsigned, v); }
DI float blo(unsigned u) { return __uint_as_float(u << 16); }
DI float bhi(unsigned u) { return __uint_as_float(u & 0xffff0000u); }
DI float dot2(unsigned a, unsigned b, float c) { return __builtin_amdgcn_fdot2_f32_bf16(__builtin_bit_cast(bf16x2, a), __builtin_bit_cast(bf16x2, b), c, false); }
DI float vcos(float r) { return __builtin_amdgcn_cosf(r); }
DI float vsin(float r) { return __builtin_amdgcn_sinf(r); }
DI float fract(float r) { return __builtin_amdgcn_fractf(r); }
DI void bar() { asm volatile("s_waitcnt lgkmcnt(0)\n\ts_barrier" ::: "memory"); }
DI float sumq(float v) {
    auto a = __builtin_amdgcn_permlane16_swap(__float_as_uint(v), __float_as_uint(v), false, false);
    v = __uint_as_float(a[0]) + __uint_as_float(a[1]);
    auto b = __builtin_amdgcn_permlane32_swap(__float_as_uint(v), __float_as_uint(v), false, false);
    return __uint_as_float(b[0]) + __uint_as_float(b[1]);
}
DI int wave_id() { return __builtin_amdgcn_readfirstlane(threadIdx.x >> 6); }

constexpr float AMP = 0.05590169943749474f;
constexpr float AMP0 = 0.03952847075210474f;

struct Map { int tt, m, rb, ct, isC, g, aj; };
DI Map get_map(int id) {
    Map r;
    const int xcd = id & 7, slot = id >> 3;
    r.tt = xcd + 8 * (slot >> 4); r.m = slot & 15; r.rb = 2 * r.tt + (r.m >> 3); r.ct = r.m & 7;
    const bool aux = slot >= 24 || (slot >= 16 && ((slot >> 2) & 1) == (xcd & 1));
    r.isC = !aux;
    r.g = 20 * xcd + (slot < 16 ? slot : 16 + (slot & 3));
    r.aj = slot >= 24 ? xcd : 8 + (xcd >> 1);
    return r;
}

DI void gen_q(u32x4 (&Q)[5][5], int ct, int w, int lane) {
    const int n = lane & 15, q = lane >> 4;
#pragma unroll
    for (int mt = 0; mt < 5; ++mt) {
        const int c = 80 * ct + 16 * mt + n;
        const float pf = c == 1 ? 320.f : (float)(c >> 1);
        const float ph = ((c & 1) && c != 1) ? 0.25f : 0.f;
        const float dl = pf * (1.f / 640.f);
        const float t = pf * (float)(160 * w + 8 * q);
        const float k = floorf(t * (1.f / 640.f));
        float r = __builtin_fmaf(-k, 640.f, t) * (1.f / 640.f);
        const float d32 = fract(pf * 0.05f);
#pragma unroll
        for (int kk = 0; kk < 5; ++kk) {
            const float rb = r + ph;
            float v[8];
#pragma unroll
            for (int j = 0; j < 8; ++j) v[j] = vcos(__builtin_fmaf((float)j, dl, rb));
            Q[mt][kk] = (u32x4){pk(v[0], v[1]), pk(v[2], v[3]), pk(v[4], v[5]), pk(v[6], v[7])};
            r = fract(r + d32);
        }
    }
}

DI void store_xh(const f32x4 (&v)[5], rsrc_t rws, unsigned o_xh, unsigned o_st, int rb, int ct, int t, int lane) {
    const int n = lane & 15, q = lane >> 4;
    float ss = 0.f;
#pragma unroll
    for (int mt = 0; mt < 5; ++mt) {
        const unsigned d0 = pk(v[mt][0], v[mt][1]), d1 = pk(v[mt][2], v[mt][3]);
        const unsigned m0 = (mt == 0 && ct == 0 && q == 0) ? (d0 & 0xffff0000u) : d0;
        ss = dot2(m0, m0, ss); ss = dot2(d1, d1, ss);
        const unsigned g = 20 * ct + 4 * mt + q;
        st8(rws, o_xh + g * 16384u + (2 * rb + (t >> 1)) * 256u + n * 16u + (t & 1) * 8u, (u32x2){d0, d1});
    }
    ss = sumq(ss);
    if (q == 0) {
        const int a = rb, h = t >> 1, half = t & 1;
        const unsigned ln = ((a >> 2) & 1) * 32 + h * 16 + n;
        st2(rws, o_st + (((ct >> 1) * 4 + (a >> 3)) * 64 + ln) * 32u + (a & 3) * 8u + half * 4u + (ct & 1) * 2u, pk(ss, 0.f) & 0xffffu);
    }
}

DI void phase_p0(const Params& p, int id, char* smem) {
    const Map mp = get_map(id);
    const int w = wave_id(), lane = threadIdx.x & 63, n = lane & 15, q = lane >> 4;
    const rsrc_t rx = mkrs(p.x, S * D * 2), rws = mkrs(p.ws, WS_USED);
    u32x4 xf[4][5];
#pragma unroll
    for (int i = 0; i < 4; ++i)
#pragma unroll
        for (int kk = 0; kk < 5; ++kk)
            xf[i][kk] = ld16(rx, (64 * mp.rb + 16 * ((w - 1 - i) & 3) + n) * 1280u + (160 * w + 32 * kk + 8 * q) * 2u);
    {
        const rsrc_t rwr = mkrs(p.wr, S * 321 * 2), rwi = mkrs(p.wi, S * 321 * 2);
        for (int e = threadIdx.x; e < 640; e += 256) {
            const int ql = e / 40, pc = 40 * mp.ct + e % 40, quad = 16 * mp.rb + ql;
            const int kh = quad >> 8, mt = (quad >> 6) & 3, qq = (quad >> 4) & 3, i = quad & 15;
            u32x4 o;
#pragma unroll
            for (int jj = 0; jj < 4; ++jj) {
                const int ks = 16 * kh + i + 32 * (16 * mt + 4 * qq + jj);
                float a, b;
                if (pc != 0) {
                    a = blo(ld2(rwr, (ks * 321 + pc) * 2u)) * (1.f / 2048.f);
                    b = blo(ld2(rwi, (ks * 321 + pc) * 2u)) * (1.f / 2048.f);
                } else {
                    const int kn = (2048 - ks) & 2047;
                    a = (blo(ld2(rwr, (ks * 321 + 320) * 2u)) + blo(ld2(rwr, (kn * 321 + 320) * 2u))) * (1.f / 4096.f);
                    b = (blo(ld2(rwi, (ks * 321 + 320) * 2u)) - blo(ld2(rwi, (kn * 321 + 320) * 2u))) * (1.f / 4096.f);
                }
                o[jj] = pk(a, b);
            }
            st16(rws, O_WFQ + (quad * 320 + pc) * 16u, o);
        }
    }
    if (id < 160) {
        const int c = 4 * id + w;
        const float pf = c == 1 ? 320.f : (float)(c >> 1);
        const float ph = ((c & 1) && c != 1) ? 0.25f : 0.f;
        const rsrc_t rb2 = mkrs(p.b2, D * 2);
        float s = 0.f;
#pragma unroll
        for (int i = 0; i < 10; ++i) {
            const int d = lane + 64 * i;
            const float t = pf * (float)d;
            const float k = floorf(t * (1.f / 640.f));
            const float r = __builtin_fmaf(-k, 640.f, t) * (1.f / 640.f);
            s += blo(ld2(rb2, d * 2u)) * vcos(r + ph);
        }
        s = sumq(s);
#pragma unroll
        for (int o = 1; o < 16; o <<= 1) s += __shfl_xor(s, o);
        if (lane == 0) st4(rws, O_B2P + c * 4u, __float_as_uint(s * (c < 2 ? AMP0 : AMP)));
    }
    u32x4 Q[5][5];
    gen_q(Q, mp.ct, w, lane);
    f32x4* slot = (f32x4*)smem;
    f32x4 acc[5];
#pragma unroll
    for (int i = 0; i < 4; ++i) {
#pragma unroll
        for (int mt = 0; mt < 5; ++mt) acc[mt] = (f32x4){0.f, 0.f, 0.f, 0.f};
#pragma unroll
        for (int kk = 0; kk < 5; ++kk)
#pragma unroll
            for (int mt = 0; mt < 5; ++mt) acc[mt] = mfma(Q[mt][kk], xf[i][kk], acc[mt]);
        if (i > 0) {
#pragma unroll
            for (int mt = 0; mt < 5; ++mt) acc[mt] += slot[((((w - 1) & 3) * 2 + ((i - 1) & 1)) * 5 + mt) * 64 + lane];
        }
        if (i < 3) {
#pragma unroll
            for (int mt = 0; mt < 5; ++mt) slot[((w * 2 + (i & 1)) * 5 + mt) * 64 + lane] = acc[mt];
            bar();
        }
    }
#pragma unroll
    for (int mt = 0; mt < 5; ++mt) {
        acc[mt] *= AMP;
        if (mt == 0 && mp.ct == 0 && q == 0) { acc[0][0] *= AMP0 / AMP; acc[0][1] *= AMP0 / AMP; }
    }
    store_xh(acc, rws, O_XH0, O_ST0, mp.rb, mp.ct, w, lane);
}

DI void phase_aux(const Params& p, int ct, int aj, char* smem) {
    const int w = wave_id(), lane = threadIdx.x & 63, n = lane & 15, q = lane >> 4;
    const rsrc_t rw1 = mkrs(p.w1, D * F * 2), rw2 = mkrs(p.w2, D * F * 2), rws = mkrs(p.ws, WS_USED);
    u32x4 Q[5][5];
    gen_q(Q, ct, w, lane);
    f32x4* slot = (f32x4*)smem;
    u32x4 I0, I1;
#pragma unroll
    for (int dd = 0; dd < 4; ++dd) {
        const int k = 8 * q + 2 * dd;
        I0[dd] = (k == n ? 0x3f80u : 0u) | (k + 1 == n ? 0x3f800000u : 0u);
        I1[dd] = (k == n + 16 ? 0x3f80u : 0u) | (k + 1 == n + 16 ? 0x3f800000u : 0u);
    }
#pragma unroll 1
    for (int i = 0; i < 4; ++i) {
        const int it = (w - 1 - i) & 3;
        f32x4 acc[2][5];
#pragma unroll
        for (int e = 0; e < 2; ++e)
#pragma unroll
            for (int mt = 0; mt < 5; ++mt) acc[e][mt] = (f32x4){0.f, 0.f, 0.f, 0.f};
#pragma unroll
        for (int kk = 0; kk < 5; ++kk) {
            const unsigned d = 160 * w + 32 * kk + 8 * (n >> 2) + (n & 3);
            const u32x4 ax = ld16(rw1, d * (F * 2) + (128 * aj + 32 * it + 8 * q) * 2u);
            const u32x4 ay = ld16(rw1, (d + 4) * (F * 2) + (128 * aj + 32 * it + 8 * q) * 2u);
            const f32x4 z = {0.f, 0.f, 0.f, 0.f};
            const f32x4 x0 = mfma(ax, I0, z), x1 = mfma(ax, I1, z), y0 = mfma(ay, I0, z), y1 = mfma(ay, I1, z);
            const u32x4 b0 = {pk(x0[0], x0[1]), pk(x0[2], x0[3]), pk(y0[0], y0[1]), pk(y0[2], y0[3])};
            const u32x4 b1 = {pk(x1[0], x1[1]), pk(x1[2], x1[3]), pk(y1[0], y1[1]), pk(y1[2], y1[3])};
#pragma unroll
            for (int mt = 0; mt < 5; ++mt) { acc[0][mt] = mfma(Q[mt][kk], b0, acc[0][mt]); acc[1][mt] = mfma(Q[mt][kk], b1, acc[1][mt]); }
        }
        if (i > 0) {
#pragma unroll
            for (int e = 0; e < 2; ++e)
#pragma unroll
                for (int mt = 0; mt < 5; ++mt) acc[e][mt] += slot[((((w - 1) & 3) * 2 + ((i - 1) & 1)) * 10 + e * 5 + mt) * 64 + lane];
        }
        if (i < 3) {
#pragma unroll
            for (int e = 0; e < 2; ++e)
#pragma unroll
                for (int mt = 0; mt < 5; ++mt) slot[((w * 2 + (i & 1)) * 10 + e * 5 + mt) * 64 + lane] = acc[e][mt];
        } else {
#pragma unroll
            for (int e = 0; e < 2; ++e)
#pragma unroll
                for (int mt = 0; mt < 5; ++mt) {
                    f32x4 v = acc[e][mt] * AMP;
                    if (mt == 0 && ct == 0 && q == 0) { v[0] = 0.f; v[1] *= AMP0 / AMP; }
                    const unsigned ft = 8 * aj + 2 * it + e, T = 5 * ct + mt;
                    st8(rws, O_W1F + ((ft * 20 + (T >> 1)) * 64 + lane) * 16u + (T & 1) * 8u, (u32x2){pk(v[0], v[1]), pk(v[2], v[3])});
                }
        }
        bar();
    }
#pragma unroll 1
    for (int i = 0; i < 4; ++i) {
        const int it = (w - 1 - i) & 3;
        f32x4 acc[2][5];
#pragma unroll
        for (int e = 0; e < 2; ++e)
#pragma unroll
            for (int mt = 0; mt < 5; ++mt) acc[e][mt] = (f32x4){0.f, 0.f, 0.f, 0.f};
        u32x4 a[2][5];
#pragma unroll
        for (int e = 0; e < 2; ++e)
#pragma unroll
            for (int kk = 0; kk < 5; ++kk)
                a[e][kk] = ld16(rw2, (128 * aj + 32 * it + 16 * e + n) * 1280u + (160 * w + 32 * kk + 8 * q) * 2u);
#pragma unroll
        for (int kk = 0; kk < 5; ++kk)
#pragma unroll
            for (int mt = 0; mt < 5; ++mt) { acc[0][mt] = mfma(a[0][kk], Q[mt][kk], acc[0][mt]); acc[1][mt] = mfma(a[1][kk], Q[mt][kk], acc[1][mt]); }
#pragma unroll
        for (int mt = 0; mt < 5; ++mt) {
            const int T = 5 * ct + mt, kg = T >> 1;
            if (kg / 5 == w) {
                const int kl = kg % 5;
                u32x4 s0 = a[0][0], s1 = a[1][0];
#pragma unroll
                for (int kk = 1; kk < 5; ++kk) if (kl == kk) { s0 = a[0][kk]; s1 = a[1][kk]; }
                const u32x4 I = (T & 1) ? I1 : I0;
                const f32x4 z = {0.f, 0.f, 0.f, 0.f};
                const f32x4 t0 = mfma(s0, I, z), t1 = mfma(s1, I, z);
                st16(rws, O_W2T + ((T * 48 + 4 * aj + it) * 64 + lane) * 16u, (u32x4){pk(t0[0], t0[1]), pk(t0[2], t0[3]), pk(t1[0], t1[1]), pk(t1[2], t1[3])});
            }
        }
        if (i > 0) {
#pragma unroll
            for (int e = 0; e < 2; ++e)
#pragma unroll
                for (int mt = 0; mt < 5; ++mt) acc[e][mt] += slot[((((w - 1) & 3) * 2 + ((i - 1) & 1)) * 10 + e * 5 + mt) * 64 + lane];
        }
        if (i < 3) {
#pragma unroll
            for (int e = 0; e < 2; ++e)
#pragma unroll
                for (int mt = 0; mt < 5; ++mt) slot[((w * 2 + (i & 1)) * 10 + e * 5 + mt) * 64 + lane] = acc[e][mt];
        } else {
#pragma unroll
            for (int mt = 0; mt < 5; ++mt) {
                const float am = (mt == 0 && ct == 0 && n < 2) ? AMP0 : AMP;
                const f32x4 v0 = acc[0][mt] * am, v1 = acc[1][mt] * am;
                st16(rws, O_W2F + (((5 * ct + mt) * 48 + 4 * aj + it) * 64 + lane) * 16u, (u32x4){pk(v0[0], v0[1]), pk(v0[2], v0[3]), pk(v1[0], v1[1]), pk(v1[2], v1[3])});
            }
        }
        bar();
    }
}

DI void phase_c(const Params& p, int g, unsigned o_in, unsigned o_st, unsigned o_out, char* smem) {
    const int w = wave_id(), lane = threadIdx.x & 63, n = lane & 15, q = lane >> 4;
    const rsrc_t rws = mkrs(p.ws, WS_USED);
    const int c = w >> 1, kh = w & 1;
    u32x4 xin[4], st[8], wf[4];
    {
        const int h = q & 1, hi = q >> 1;
#pragma unroll
        for (int ii = 0; ii < 4; ++ii) xin[ii] = ld16(rws, o_in + g * 16384u + (2 * (8 * w + 4 * hi + ii) + h) * 256u + n * 16u);
#pragma unroll
        for (int cp = 0; cp < 4; ++cp)
#pragma unroll
            for (int e = 0; e < 2; ++e) st[cp * 2 + e] = ld16(rws, o_st + ((cp * 4 + w) * 64 + lane) * 32u + e * 16u);
#pragma unroll
        for (int mt = 0; mt < 4; ++mt) wf[mt] = ld16(rws, O_WFQ + ((((kh * 4 + mt) * 4 + q) * 16 + n) * 320 + 2 * g + c) * 16u);
    }
    u32x4 B1r[2], B1i[2], A3[4][4], A6[2][2];
    f32x2 twc[4][2], tws[4][2];
    {
        const float ka = (float)(16 * kh + n);
#pragma unroll
        for (int ks = 0; ks < 2; ++ks)
#pragma unroll
            for (int m = 0; m < 4; ++m) {
                const float r = fract((float)(16 * ks + m) * (1.f / 32.f) * ka + (float)(4 * q) * (1.f / 32.f) * ka);
                const float cs = vcos(r), sn = vsin(r);
                B1r[ks][m] = pk(cs, sn); B1i[ks][m] = pk(-sn, cs);
            }
#pragma unroll
        for (int bt = 0; bt < 4; ++bt)
#pragma unroll
            for (int r = 0; r < 4; ++r) {
                const float a = (float)(16 * bt + 4 * q + r) * ka * (1.f / 2048.f);
                twc[bt][r >> 1][r & 1] = vcos(a); tws[bt][r >> 1][r & 1] = vsin(a);
            }
#pragma unroll
        for (int bt = 0; bt < 4; ++bt)
#pragma unroll
            for (int m = 0; m < 4; ++m) {
                const float a = (float)n * (float)(16 * bt + 4 * q + m) * (1.f / 64.f);
                const float cs = vcos(a), sn = vsin(a);
#pragma unroll
                for (int mt = 0; mt < 4; ++mt) {
                    const int k4 = (mt * m) & 3;
                    A3[mt][bt][m] = k4 == 0 ? pk(cs, sn) : k4 == 1 ? pk(-sn, cs) : k4 == 2 ? pk(-cs, -sn) : pk(sn, -cs);
                }
            }
        const float rio = (float)(n & 1) * -0.25f;
#pragma unroll
        for (int rt = 0; rt < 2; ++rt)
#pragma unroll
            for (int ks = 0; ks < 2; ++ks)
#pragma unroll
                for (int m = 0; m < 4; ++m) {
                    const float a = (float)(16 * (w >> 1) + 8 * rt + (n >> 1)) * (float)(16 * ks + 4 * q + m) * (1.f / 32.f) + rio;
                    A6[rt][ks][m] = pk(vcos(a), vcos(a + 0.25f));
                }
    }
    u32x4* FR = (u32x4*)smem;
    u32x4* RAW = (u32x4*)(smem + 16384);
    {
        const int h = q & 1, hi = q >> 1;
        float rs[4][2];
#pragma unroll
        for (int ii = 0; ii < 4; ++ii)
#pragma unroll
            for (int hf = 0; hf < 2; ++hf) {
                float s = 0.f;
#pragma unroll
                for (int cp = 0; cp < 4; ++cp) s = dot2(st[cp * 2 + (ii >> 1)][(ii & 1) * 2 + hf], 0x3f803f80u, s);
                rs[ii][hf] = __builtin_amdgcn_rsqf(__builtin_fmaf(s, 1.f / 640.f, 1e-5f));
            }
#pragma unroll
        for (int ii = 0; ii < 4; ++ii) RAW[((8 * w + 4 * hi + ii) * 2 + h) * 16 + n] = xin[ii];
        const int aq = 2 * w + hi;
#pragma unroll
        for (int hf = 0; hf < 2; ++hf)
#pragma unroll
            for (int cc = 0; cc < 2; ++cc) {
                u32x4 o;
#pragma unroll
                for (int ii = 0; ii < 4; ++ii) {
                    const unsigned u = xin[ii][hf * 2 + cc];
                    float re = blo(u) * rs[ii][hf];
                    if (cc == 0 && g == 0) re = 0.f;
                    o[ii] = pk(re, bhi(u) * rs[ii][hf]);
                }
                FR[((cc * 4 + 2 * h + hf) * 2 + (aq >> 2)) * 64 + (aq & 3) * 16 + n] = o;
            }
    }
    bar();
    f32x4 tr[4], ti[4];
#pragma unroll
    for (int bt = 0; bt < 4; ++bt) {
        const u32x4 a0 = FR[((c * 4 + bt) * 2 + 0) * 64 + lane], a1 = FR[((c * 4 + bt) * 2 + 1) * 64 + lane];
        const f32x4 z = {0.f, 0.f, 0.f, 0.f};
        tr[bt] = mfma(a1, B1r[1], mfma(a0, B1r[0], z));
        ti[bt] = mfma(a1, B1i[1], mfma(a0, B1i[0], z));
    }
    bar();
    u32x4 P[4], Pn[4];
#pragma unroll
    for (int bt = 0; bt < 4; ++bt)
#pragma unroll
        for (int r2 = 0; r2 < 2; ++r2) {
            const f32x2 a = {tr[bt][2 * r2], tr[bt][2 * r2 + 1]}, b = {ti[bt][2 * r2], ti[bt][2 * r2 + 1]};
            const f32x2 xr = a * twc[bt][r2] + b * tws[bt][r2], xi = b * twc[bt][r2] - a * tws[bt][r2];
            P[bt][2 * r2] = pk(xr[0], xi[0]); P[bt][2 * r2 + 1] = pk(xr[1], xi[1]);
            Pn[bt][2 * r2] = pk(xi[0], -xr[0]); Pn[bt][2 * r2 + 1] = pk(xi[1], -xr[1]);
        }
    u32x4 R[4], Rn[4];
#pragma unroll
    for (int mt = 0; mt < 4; ++mt) {
        f32x4 zr = {0.f, 0.f, 0.f, 0.f}, zi = {0.f, 0.f, 0.f, 0.f};
#pragma unroll
        for (int bt = 0; bt < 4; ++bt) { zr = mfma(A3[mt][bt], P[bt], zr); zi = mfma(A3[mt][bt], Pn[bt], zi); }
        unsigned pr[4], pi[4];
#pragma unroll
        for (int r = 0; r < 4; ++r) {
            const float fr = blo(wf[mt][r]), fi = bhi(wf[mt][r]);
            const float yr = zr[r] * fr - zi[r] * fi, yi = zr[r] * fi + zi[r] * fr;
            pr[r] = pk(yr, -yi); pi[r] = pk(yi, yr);
        }
        R[mt] = (u32x4){pr[0], pr[1], pr[2], pr[3]}; Rn[mt] = (u32x4){pi[0], pi[1], pi[2], pi[3]};
    }
    unsigned* TR = (unsigned*)smem;
#pragma unroll
    for (int bt = 0; bt < 4; ++bt) {
        f32x4 ur = {0.f, 0.f, 0.f, 0.f}, ui = {0.f, 0.f, 0.f, 0.f};
#pragma unroll
        for (int kt = 0; kt < 4; ++kt) { ur = mfma(A3[bt][kt], R[kt], ur); ui = mfma(A3[bt][kt], Rn[kt], ui); }
#pragma unroll
        for (int r2 = 0; r2 < 2; ++r2) {
            const f32x2 a = {ur[2 * r2], ur[2 * r2 + 1]}, b = {ui[2 * r2], ui[2 * r2 + 1]};
            const f32x2 xr = a * twc[bt][r2] - b * tws[bt][r2], xi = b * twc[bt][r2] + a * tws[bt][r2];
#pragma unroll
            for (int e = 0; e < 2; ++e) {
                const int r = 2 * r2 + e;
                const int b_ = 16 * bt + 4 * q + r;
                const int kq = 4 * kh + (n >> 2), f = (2 * q + r2) & 7;
                TR[c * 2048 + b_ * 32 + ((kq ^ f) << 2) + (n & 3)] = pk(xr[e], xi[e]);
            }
        }
    }
    bar();
    {
        const int h = w & 1, ah = w >> 1;
        const u32x4* TRv = (const u32x4*)smem;
        u32x4 raw[2][2];
#pragma unroll
        for (int rt = 0; rt < 2; ++rt)
#pragma unroll
            for (int e = 0; e < 2; ++e) raw[rt][e] = RAW[((16 * ah + 8 * rt + 2 * q + e) * 2 + h) * 16 + n];
        u32x4 o[2][2];
#pragma unroll
        for (int hf = 0; hf < 2; ++hf)
#pragma unroll
            for (int cc = 0; cc < 2; ++cc) {
                const int b_ = 16 * (2 * h + hf) + n, f = (n >> 1) & 7;
                const u32x4 b0 = TRv[cc * 512 + b_ * 8 + ((0 + q) ^ f)], b1 = TRv[cc * 512 + b_ * 8 + ((4 + q) ^ f)];
#pragma unroll
                for (int rt = 0; rt < 2; ++rt) {
                    const unsigned r0 = raw[rt][0][hf * 2 + cc], r1 = raw[rt][1][hf * 2 + cc];
                    f32x4 y = {0.f, 0.f, 0.f, 0.f};
                    y = mfma(A6[rt][0], b0, y); y = mfma(A6[rt][1], b1, y);
                    if (cc == 0 && g == 0) { y[0] = 0.f; y[2] = 0.f; }
                    o[rt][0][hf * 2 + cc] = pk(y[0] + blo(r0), y[1] + bhi(r0));
                    o[rt][1][hf * 2 + cc] = pk(y[2] + blo(r1), y[3] + bhi(r1));
                }
            }
#pragma unroll
        for (int rt = 0; rt < 2; ++rt)
#pragma unroll
            for (int e = 0; e < 2; ++e)
                st16(rws, o_out + g * 16384u + (2 * (16 * ah + 8 * rt + 2 * q + e) + h) * 256u + n * 16u, o[rt][e]);
    }
}

DI void phase_g1(const Params& p, int tt, int fs, unsigned o_in, unsigned o_hf, char* smem) {
    const int w = wave_id(), lane = threadIdx.x & 63, n = lane & 15, q = lane >> 4;
    const rsrc_t rws = mkrs(p.ws, WS_USED), rb1 = mkrs(p.b1, F * 2);
#pragma unroll
    for (int i = 0; i < 30; ++i) {
        const int ch = w * 30 + i;
        __builtin_amdgcn_global_load_lds((const GAS unsigned*)(p.ws + O_W1F + (size_t)fs * 122880 + ch * 1024 + lane * 16), (LAS unsigned*)(smem + ch * 1024), 16, 0, 0);
    }
    const int blk = 4 * tt + w;
    u32x4 l[20][2];
#pragma unroll
    for (int kk = 0; kk < 20; ++kk)
#pragma unroll
        for (int e = 0; e < 2; ++e) l[kk][e] = ld16(rws, o_in + (8 * kk + 4 * e + q) * 16384u + blk * 256u + n * 16u);
    u32x2 bb[6];
#pragma unroll
    for (int mt = 0; mt < 6; ++mt) bb[mt] = ld8(rb1, (96 * fs + 16 * mt + 4 * q) * 2u);
    asm volatile("s_waitcnt vmcnt(46)" ::: "memory");
    bar();
    const u32x4* WL = (const u32x4*)smem;
    f32x4 acc[6][2];
#pragma unroll
    for (int mt = 0; mt < 6; ++mt) { acc[mt][0] = (f32x4){0.f, 0.f, 0.f, 0.f}; acc[mt][1] = (f32x4){0.f, 0.f, 0.f, 0.f}; }
    float ss0 = 0.f, ss1 = 0.f;
#pragma unroll
    for (int kk = 0; kk < 20; ++kk) {
        const u32x4 f0 = {l[kk][0][0], l[kk][0][1], l[kk][1][0], l[kk][1][1]};
        const u32x4 f1 = {l[kk][0][2], l[kk][0][3], l[kk][1][2], l[kk][1][3]};
#pragma unroll
        for (int mt = 0; mt < 6; ++mt) {
            const u32x4 a = WL[(mt * 20 + kk) * 64 + lane];
            acc[mt][0] = mfma(a, f0, acc[mt][0]); acc[mt][1] = mfma(a, f1, acc[mt][1]);
        }
#pragma unroll
        for (int dd = 0; dd < 4; ++dd) {
            unsigned u0 = f0[dd], u1 = f1[dd];
            if (kk == 0 && dd == 0) { u0 = q == 0 ? (u0 & 0xffff0000u) : u0; u1 = q == 0 ? (u1 & 0xffff0000u) : u1; }
            ss0 = dot2(u0, u0, ss0); ss1 = dot2(u1, u1, ss1);
        }
    }
    float rs[2];
    rs[0] = __builtin_amdgcn_rsqf(__builtin_fmaf(sumq(ss0), 1.f / 640.f, 1e-5f));
    rs[1] = __builtin_amdgcn_rsqf(__builtin_fmaf(sumq(ss1), 1.f / 640.f, 1e-5f));
    constexpr float C0 = -2.3022081985f, C1 = -0.1029432397f;
#pragma unroll
    for (int hf = 0; hf < 2; ++hf)
#pragma unroll
        for (int u = 0; u < 3; ++u) {
            u32x4 o;
#pragma unroll
            for (int e = 0; e < 2; ++e) {
                const int mt = 2 * u + e;
                float hv[4];
#pragma unroll
                for (int r = 0; r < 4; ++r) {
                    const float bv = (r & 1) ? bhi(bb[mt][r >> 1]) : blo(bb[mt][r >> 1]);
                    const float x = __builtin_fmaf(acc[mt][hf][r], rs[hf], bv);
                    const float ex = __builtin_amdgcn_exp2f(x * __builtin_fmaf(x * x, C1, C0));
                    hv[r] = x * __builtin_amdgcn_rcpf(1.f + ex);
                }
                o[2 * e] = pk(hv[0], hv[1]); o[2 * e + 1] = pk(hv[2], hv[3]);
            }
            st16(rws, o_hf + (((2 * blk + hf) * 48 + 3 * fs + u) * 64 + lane) * 16u, o);
        }
}

DI void phase_g2(const Params& p, int rb, int ct, unsigned o_hf, unsigned o_in, unsigned o_out, unsigned o_st, char* smem) {
    const int w = wave_id(), lane = threadIdx.x & 63, n = lane & 15, q = lane >> 4;
    const rsrc_t rws = mkrs(p.ws, WS_USED);
    u32x4 W[5][12];
#pragma unroll
    for (int u = 0; u < 12; ++u)
#pragma unroll
        for (int mt = 0; mt < 5; ++mt) W[mt][u] = ld16(rws, O_W2F + (((5 * ct + mt) * 48 + 12 * w + u) * 64 + lane) * 16u);
    u32x4 hf[2][12];
#pragma unroll
    for (int i = 0; i < 2; ++i)
#pragma unroll
        for (int u = 0; u < 12; ++u) hf[i][u] = ld16(rws, o_hf + (((4 * rb + ((w - 1 - i) & 3)) * 48 + 12 * w + u) * 64 + lane) * 16u);
    u32x2 res[5]; f32x4 bv[5];
#pragma unroll
    for (int mt = 0; mt < 5; ++mt) {
        res[mt] = ld8(rws, o_in + (20 * ct + 4 * mt + q) * 16384u + (2 * rb + (w >> 1)) * 256u + n * 16u + (w & 1) * 8u);
        bv[mt] = __builtin_bit_cast(f32x4, ld16(rws, O_B2P + (80 * ct + 16 * mt + 4 * q) * 4u));
    }
    f32x4* slot = (f32x4*)smem;
    f32x4 acc[5];
#pragma unroll
    for (int i = 0; i < 4; ++i) {
#pragma unroll
        for (int mt = 0; mt < 5; ++mt) acc[mt] = (f32x4){0.f, 0.f, 0.f, 0.f};
#pragma unroll
        for (int u = 0; u < 12; ++u) {
#pragma unroll
            for (int mt = 0; mt < 5; ++mt) acc[mt] = mfma(W[mt][u], hf[i & 1][u], acc[mt]);
            if (i < 2) hf[i & 1][u] = ld16(rws, o_hf + (((4 * rb + ((w - 3 - i) & 3)) * 48 + 12 * w + u) * 64 + lane) * 16u);
        }
        if (i > 0) {
#pragma unroll
            for (int mt = 0; mt < 5; ++mt) acc[mt] += slot[((((w - 1) & 3) * 2 + ((i - 1) & 1)) * 5 + mt) * 64 + lane];
        }
        if (i < 3) {
#pragma unroll
            for (int mt = 0; mt < 5; ++mt) slot[((w * 2 + (i & 1)) * 5 + mt) * 64 + lane] = acc[mt];
            bar();
        }
    }
#pragma unroll
    for (int mt = 0; mt < 5; ++mt) {
        acc[mt] += bv[mt];
        acc[mt][0] += blo(res[mt][0]); acc[mt][1] += bhi(res[mt][0]); acc[mt][2] += blo(res[mt][1]); acc[mt][3] += bhi(res[mt][1]);
    }
    store_xh(acc, rws, o_out, o_st, rb, ct, w, lane);
}

DI void phase_g4(const Params& p, int rb, int ct, unsigned o_hf, unsigned o_in, char* smem) {
    const int w = wave_id(), lane = threadIdx.x & 63, n = lane & 15, q = lane >> 4;
    const rsrc_t rws = mkrs(p.ws, WS_USED), rb2 = mkrs(p.b2, D * 2), rout = mkrs(p.out, S * D * 2);
    f32x4* slot = (f32x4*)smem;
    f32x4* xp = (f32x4*)(smem + 40960);
    {
        u32x4 xf[4][5];
#pragma unroll
        for (int t = 0; t < 4; ++t)
#pragma unroll
            for (int kk = 0; kk < 5; ++kk) {
                const unsigned g0 = 40 * w + 8 * kk + q;
                const u32x2 a = ld8(rws, o_in + g0 * 16384u + (2 * rb + (t >> 1)) * 256u + n * 16u + (t & 1) * 8u);
                const u32x2 b = ld8(rws, o_in + (g0 + 4) * 16384u + (2 * rb + (t >> 1)) * 256u + n * 16u + (t & 1) * 8u);
                xf[t][kk] = (u32x4){a[0], a[1], b[0], b[1]};
            }
        u32x4 Q[5][5];
#pragma unroll
        for (int kk = 0; kk < 5; ++kk)
#pragma unroll
            for (int hh = 0; hh < 2; ++hh)
#pragma unroll
                for (int pr = 0; pr < 2; ++pr) {
                    const int c = 160 * w + 32 * kk + 16 * hh + 4 * q + 2 * pr;
                    const float pf = (float)(c >> 1);
#pragma unroll
                    for (int mt = 0; mt < 5; ++mt) {
                        const float df = (float)(80 * ct + 16 * mt + n);
                        const float t = pf * df;
                        const float k = floorf(t * (1.f / 640.f));
                        const float r = __builtin_fmaf(-k, 640.f, t) * (1.f / 640.f);
                        float v0 = vcos(r) * AMP, v1 = vcos(r + 0.25f) * AMP;
                        if (kk == 0 && hh == 0 && pr == 0 && c == 0) { v0 = AMP0; v1 = (n & 1) ? -AMP0 : AMP0; }
                        Q[mt][kk][2 * hh + pr] = pk(v0, v1);
                    }
                }
#pragma unroll
        for (int t = 0; t < 4; ++t) {
            f32x4 a[5];
#pragma unroll
            for (int mt = 0; mt < 5; ++mt) a[mt] = (f32x4){0.f, 0.f, 0.f, 0.f};
#pragma unroll
            for (int kk = 0; kk < 5; ++kk)
#pragma unroll
                for (int mt = 0; mt < 5; ++mt) a[mt] = mfma(Q[mt][kk], xf[t][kk], a[mt]);
#pragma unroll
            for (int mt = 0; mt < 5; ++mt) xp[((w * 4 + t) * 5 + mt) * 64 + lane] = a[mt];
        }
    }
    u32x4 W[5][12];
#pragma unroll
    for (int u = 0; u < 12; ++u)
#pragma unroll
        for (int mt = 0; mt < 5; ++mt) W[mt][u] = ld16(rws, O_W2T + (((5 * ct + mt) * 48 + 12 * w + u) * 64 + lane) * 16u);
    u32x4 hf[2][12];
#pragma unroll
    for (int i = 0; i < 2; ++i)
#pragma unroll
        for (int u = 0; u < 12; ++u) hf[i][u] = ld16(rws, o_hf + (((4 * rb + ((w - 1 - i) & 3)) * 48 + 12 * w + u) * 64 + lane) * 16u);
    u32x2 bb[5];
#pragma unroll
    for (int mt = 0; mt < 5; ++mt) bb[mt] = ld8(rb2, (80 * ct + 16 * mt + 4 * q) * 2u);
    f32x4 acc[5];
#pragma unroll
    for (int i = 0; i < 4; ++i) {
        const int t = (w - 1 - i) & 3;
#pragma unroll
        for (int mt = 0; mt < 5; ++mt) acc[mt] = xp[((w * 4 + t) * 5 + mt) * 64 + lane];
#pragma unroll
        for (int u = 0; u < 12; ++u) {
#pragma unroll
            for (int mt = 0; mt < 5; ++mt) acc[mt] = mfma(W[mt][u], hf[i & 1][u], acc[mt]);
            if (i < 2) hf[i & 1][u] = ld16(rws, o_hf + (((4 * rb + ((w - 3 - i) & 3)) * 48 + 12 * w + u) * 64 + lane) * 16u);
        }
        if (i > 0) {
#pragma unroll
            for (int mt = 0; mt < 5; ++mt) acc[mt] += slot[((((w - 1) & 3) * 2 + ((i - 1) & 1)) * 5 + mt) * 64 + lane];
        }
        if (i < 3) {
#pragma unroll
            for (int mt = 0; mt < 5; ++mt) slot[((w * 2 + (i & 1)) * 5 + mt) * 64 + lane] = acc[mt];
            bar();
        }
    }
#pragma unroll
    for (int mt = 0; mt < 5; ++mt) {
        const unsigned d0 = pk(acc[mt][0] + blo(bb[mt][0]), acc[mt][1] + bhi(bb[mt][0]));
        const unsigned d1 = pk(acc[mt][2] + blo(bb[mt][1]), acc[mt][3] + bhi(bb[mt][1]));
        st8(rout, (64 * rb + 16 * w + n) * 1280u + (80 * ct + 16 * mt + 4 * q) * 2u, (u32x2){d0, d1});
    }
}

__global__ void __launch_bounds__(256, 1) k_p0(Params p) {
    __shared__ __attribute__((aligned(16))) char smem[40960];
    phase_p0(p, blockIdx.x, smem);
}
template <int L>
__global__ void __launch_bounds__(256, 1) k_c(Params p) {
    __shared__ __attribute__((aligned(16))) char smem[81920];
    const Map mp = get_map(blockIdx.x);
    if (mp.isC) phase_c(p, mp.g, L ? O_XH2 : O_XH0, L ? O_ST2 : O_ST0, L ? O_XH3 : O_XH1, smem);
    else if (L == 0) phase_aux(p, mp.ct, mp.aj, smem);
}
template <int L>
__global__ void __launch_bounds__(256, 1) k_g1(Params p) {
    __shared__ __attribute__((aligned(16))) char smem[122880];
    const Map mp = get_map(blockIdx.x);
    phase_g1(p, mp.tt, mp.m, L ? O_XH3 : O_XH1, L ? O_HF2 : O_HF1, smem);
}
__global__ void __launch_bounds__(256, 1) k_g2(Params p) {
    __shared__ __attribute__((aligned(16))) char smem[40960];
    const Map mp = get_map(blockIdx.x);
    phase_g2(p, mp.rb, mp.ct, O_HF1, O_XH1, O_XH2, O_ST2, smem);
}
__global__ void __launch_bounds__(256, 1) k_g4(Params p) {
    __shared__ __attribute__((aligned(16))) char smem[122880];
    const Map mp = get_map(blockIdx.x);
    phase_g4(p, mp.rb, mp.ct, O_HF2, O_XH3, smem);
}

extern "C" void kernel_launch(void* const* d_in, const int* in_sizes, int n_in, void* d_out, int out_size, void* d_ws, size_t ws_size,
                              hipStream_t stream) {
    Params p;
    p.x = (const uint16_t*)d_in[0]; p.wr = (const uint16_t*)d_in[1]; p.wi = (const uint16_t*)d_in[2];
    p.w1 = (const uint16_t*)d_in[4]; p.b1 = (const uint16_t*)d_in[5]; p.w2 = (const uint16_t*)d_in[6]; p.b2 = (const uint16_t*)d_in[7];
    p.out = (uint16_t*)d_out; p.ws = (char*)d_ws;
    k_p0<<<256, 256, 0, stream>>>(p);
    k_c<0><<<256, 256, 0, stream>>>(p);
    k_g1<0><<<256, 256, 0, stream>>>(p);
    k_g2<<<256, 256, 0, stream>>>(p);
    k_c<1><<<256, 256, 0, stream>>>(p);
    k_g1<1><<<256, 256, 0, stream>>>(p);
    k_g4<<<256, 256, 0, stream>>>(p);
}
```

```cpp
#include <hip/hip_runtime.h>
#include <stdint.h>

typedef float f32x4 __attribute__((ext_vector_type(4)));
typedef float f32x2 __attribute__((ext_vector_type(2)));
typedef unsigned u32x4 __attribute__((ext_vector_type(4)));
typedef unsigned u32x2 __attribute__((ext_vector_type(2)));
typedef __bf16 bf16x8 __attribute__((ext_vector_type(8)));
typedef __bf16 bf16x2 __attribute__((ext_vector_type(2)));
typedef __amdgpu_buffer_rsrc_t rsrc_t;
typedef unsigned long long u64;
#define DI __device__ __forceinline__
#define GAS __attribute__((address_space(1)))
#define LAS __attribute__((address_space(3)))

constexpr int S = 2048, D = 640, F = 1536;
#define GSPAD 512
constexpr unsigned GS = 16384u + GSPAD;
constexpr unsigned XH_BYTES = 160u * GS;
constexpr unsigned O_XH0 = 0, O_XH1 = XH_BYTES, O_XH2 = 2 * XH_BYTES, O_XH3 = 3 * XH_BYTES;
constexpr unsigned O_HF1 = 0x0B00000, O_HF2 = 0x1100000;
constexpr unsigned O_W1F = 0x1700000;
constexpr unsigned O_W2F = 0x1900000;
constexpr unsigned O_W2T = 0x1B00000;
constexpr unsigned O_WFQ = 0x1D00000;
constexpr unsigned O_ST0 = 0x1F80000, O_ST2 = 0x1F90000;
constexpr unsigned O_FLG = 0x1FA0000;
constexpr unsigned O_EPO = 0x1FE0000;
constexpr unsigned O_B2P = 0x2000000;
constexpr unsigned WS_USED = 0x2100000;
constexpr unsigned LDS_SCR = 122880;
enum { PH_P0 = 0, PH_A1, PH_A2, PH_C1, PH_G1A, PH_G2, PH_C2, PH_G1B };

struct Params {
    const uint16_t* x; const uint16_t* wr; const uint16_t* wi; const uint16_t* w1; const uint16_t* b1;
    const uint16_t* w2; const uint16_t* b2; uint16_t* out; char* ws;
};

#define PIN(x) asm volatile("" : "+v"(x))

DI rsrc_t mkrs(const void* p, unsigned bytes) { return __builtin_amdgcn_make_buffer_rsrc((void*)p, 0, bytes, 0x00020000); }
DI u32x4 ld16(rsrc_t r, unsigned off) { return __builtin_amdgcn_raw_buffer_load_b128(r, off, 0, 0); }
DI u32x2 ld8(rsrc_t r, unsigned off) { return __builtin_amdgcn_raw_buffer_load_b64(r, off, 0, 0); }
DI unsigned ld4(rsrc_t r, unsigned off) { return __builtin_amdgcn_raw_buffer_load_b32(r, off, 0, 0); }
DI unsigned ld2(rsrc_t r, unsigned off) { return __builtin_amdgcn_raw_buffer_load_b16(r, off, 0, 0); }
DI void st16(rsrc_t r, unsigned off, u32x4 v) { __builtin_amdgcn_raw_buffer_store_b128(v, r, off, 0, 0); }
DI void st8(rsrc_t r, unsigned off, u32x2 v) { __builtin_amdgcn_raw_buffer_store_b64(v, r, off, 0, 0); }
DI u32x4 cld16(rsrc_t r, unsigned off) { return __builtin_amdgcn_raw_buffer_load_b128(r, off, 0, 16); }
DI u32x2 cld8(rsrc_t r, unsigned off) { return __builtin_amdgcn_raw_buffer_load_b64(r, off, 0, 16); }
DI void cst16(rsrc_t r, unsigned off, u32x4 v) { __builtin_amdgcn_raw_buffer_store_b128(v, r, off, 0, 16); }
DI void cst8(rsrc_t r, unsigned off, u32x2 v) { __builtin_amdgcn_raw_buffer_store_b64(v, r, off, 0, 16); }
DI void cst4(rsrc_t r, unsigned off, unsigned v) { __builtin_amdgcn_raw_buffer_store_b32(v, r, off, 0, 16); }
DI f32x4 mfma(u32x4 a, u32x4 b, f32x4 c) {
    return __builtin_amdgcn_mfma_f32_16x16x32_bf16(__builtin_bit_cast(bf16x8, a), __builtin_bit_cast(bf16x8, b), c, 0, 0, 0);
}
DI unsigned pk(float lo, float hi) { bf16x2 v = __builtin_convertvector((f32x2){lo, hi}, bf16x2); return __builtin_bit_cast(unsigned, v); }
DI float blo(unsigned u) { return __uint_as_float(u << 16); }
DI float bhi(unsigned u) { return __uint_as_float(u & 0xffff0000u); }
DI float dot2(unsigned a, unsigned b, float c) { return __builtin_amdgcn_fdot2_f32_bf16(__builtin_bit_cast(bf16x2, a), __builtin_bit_cast(bf16x2, b), c, false); }
DI float vcos(float r) { return __builtin_amdgcn_cosf(r); }
DI float vsin(float r) { return __builtin_amdgcn_sinf(r); }
DI float fract(float r) { return __builtin_amdgcn_fractf(r); }
DI void bar() { asm volatile("s_waitcnt lgkmcnt(0)\n\ts_barrier" ::: "memory"); }
DI float sumq(float v) {
    auto a = __builtin_amdgcn_permlane16_swap(__float_as_uint(v), __float_as_uint(v), false, false);
    v = __uint_as_float(a[0]) + __uint_as_float(a[1]);
    auto b = __builtin_amdgcn_permlane32_swap(__float_as_uint(v), __float_as_uint(v), false, false);
    return __uint_as_float(b[0]) + __uint_as_float(b[1]);
}
#define SB() __builtin_amdgcn_sched_barrier(0)
DI int fresh_tid() { int t = threadIdx.x; asm volatile("" : "+v"(t)); return t; }
DI int fresh_s(int v) { asm volatile("" : "+s"(v)); return v; }
#define IDS const int tid = fresh_tid(), w = __builtin_amdgcn_readfirstlane(tid >> 6), lane = tid & 63, n = lane & 15, q = lane >> 4

DI void publish(char* ws, int ph, int idx, u64 ep) {
    asm volatile("s_waitcnt vmcnt(0)" ::: "memory");
    bar();
    if (threadIdx.x == 0) __hip_atomic_store((GAS u64*)(ws + O_FLG + (ph * 256 + idx) * 128), ep, __ATOMIC_RELAXED, __HIP_MEMORY_SCOPE_AGENT);
}
DI void wait_flags(char* ws, int ph, int idx, u64 ep) {
    GAS u64* f = (GAS u64*)(ws + O_FLG + (ph * 256 + idx) * 128);
    for (int s = 0; s < 400000; ++s) {
        const u64 v = __hip_atomic_load(f, __ATOMIC_RELAXED, __HIP_MEMORY_SCOPE_AGENT);
        if (__all(v == ep)) break;
        __builtin_amdgcn_s_sleep(1);
    }
    asm volatile("" ::: "memory");
}

constexpr float AMP = 0.05590169943749474f;
constexpr float AMP0 = 0.03952847075210474f;

struct Map { int tt, m, rb, ct, isC, g, aj; };
__host__ __device__ inline Map get_map(int id) {
    Map r;
    const int xcd = id & 7, slot = id >> 3;
    r.tt = xcd + 8 * (slot >> 4); r.m = slot & 15; r.rb = 2 * r.tt + (r.m >> 3); r.ct = r.m & 7;
    const bool aux = slot >= 24 || (slot >= 16 && ((slot >> 2) & 1) == (xcd & 1));
    r.isC = !aux;
    r.g = 20 * xcd + (slot < 16 ? slot : 16 + (slot & 3));
    r.aj = slot >= 24 ? xcd : 8 + (xcd >> 1);
    return r;
}

DI void gen_q(u32x4 (&Q)[5][5], int ct, int w, int lane) {
    const int n = lane & 15, q = lane >> 4;
#pragma unroll
    for (int mt = 0; mt < 5; ++mt) {
        const int c = 80 * ct + 16 * mt + n;
        const float pf = c == 1 ? 320.f : (float)(c >> 1);
        const float ph = ((c & 1) && c != 1) ? 0.25f : 0.f;
        const float dl = pf * (1.f / 640.f);
        const float t = pf * (float)(160 * w + 8 * q);
        const float k = floorf(t * (1.f / 640.f));
        float r = __builtin_fmaf(-k, 640.f, t) * (1.f / 640.f);
        const float d32 = fract(pf * 0.05f);
#pragma unroll
        for (int kk = 0; kk < 5; ++kk) {
            const float rb = r + ph;
            float v[8];
#pragma unroll
            for (int j = 0; j < 8; ++j) v[j] = vcos(__builtin_fmaf((float)j, dl, rb));
            Q[mt][kk] = (u32x4){pk(v[0], v[1]), pk(v[2], v[3]), pk(v[4], v[5]), pk(v[6], v[7])};
            r = fract(r + d32);
        }
    }
}

DI void store_xh(const f32x4 (&v)[5], rsrc_t rws, unsigned o_xh, unsigned o_st, int rb, int ct, int t, int lane, char* xb) {
    const int n = lane & 15, q = lane >> 4;
    u32x2 d[5];
    float ss = 0.f;
#pragma unroll
    for (int mt = 0; mt < 5; ++mt) {
        d[mt] = (u32x2){pk(v[mt][0], v[mt][1]), pk(v[mt][2], v[mt][3])};
        const unsigned m0 = (mt == 0 && ct == 0 && q == 0) ? (d[mt][0] & 0xffff0000u) : d[mt][0];
        ss = dot2(m0, m0, ss); ss = dot2(d[mt][1], d[mt][1], ss);
    }
    u32x2* mine = (u32x2*)(xb + t * 10240) + lane;
    const u32x2* other = (const u32x2*)(xb + (t ^ 1) * 10240) + lane;
    const unsigned base = o_xh + (20 * ct + q) * GS + (2 * rb + (t >> 1)) * 256u + n * 16u;
    if (t & 1) {
        mine[0] = d[0]; mine[64] = d[1]; mine[128] = d[2];
        ss = sumq(ss);
        mine[192] = (u32x2){__float_as_uint(ss), 0u};
        bar();
        const u32x2 a = other[0], b = other[64];
        cst16(rws, base + 12 * GS, (u32x4){a[0], a[1], d[3][0], d[3][1]});
        cst16(rws, base + 16 * GS, (u32x4){b[0], b[1], d[4][0], d[4][1]});
    } else {
        mine[0] = d[3]; mine[64] = d[4];
        ss = sumq(ss);
        bar();
        const u32x2 a = other[0], b = other[64], c = other[128], s1 = other[192];
        cst16(rws, base, (u32x4){d[0][0], d[0][1], a[0], a[1]});
        cst16(rws, base + 4 * GS, (u32x4){d[1][0], d[1][1], b[0], b[1]});
        cst16(rws, base + 8 * GS, (u32x4){d[2][0], d[2][1], c[0], c[1]});
        const unsigned ln = ((t >> 1) + 2 * ((rb >> 2) & 1)) * 16 + n;
        const unsigned so = o_st + ((ct * 4 + (rb >> 3)) * 64 + ln) * 16u + (rb & 3) * 4u;
        cst4(rws, q == 0 ? so : 0xfffffff0u, pk(ss, __uint_as_float(s1[0])));
    }
}

template <int NK, int NA, int BARU, int SI, class MM>
DI void ring(f32x4 (&res)[NA], f32x4* slot, int w, int lane, MM mm) {
    f32x4 acc[2][NA], in[NA];
    f32x4* wr = slot + w * (2 * NA * 64) + lane;
    const f32x4* rd = slot + ((w - 1) & 3) * (2 * NA * 64) + lane;
#pragma unroll
    for (int i = 0; i < 4; ++i) {
#pragma unroll
        for (int u = 0; u < NK; ++u) {
            if (u == 0) {
#pragma unroll
                for (int m = 0; m < NA; ++m) acc[i & 1][m] = (f32x4){0.f, 0.f, 0.f, 0.f};
            }
            mm(i, u, acc[i & 1]);
            if (i > 0 && u < BARU) {
#pragma unroll
                for (int m = u * NA / BARU; m < (u + 1) * NA / BARU; ++m) {
                    if (i > 1) acc[(i - 1) & 1][m] += in[m];
                    wr[(((i - 1) & 1) * NA + m) * 64] = acc[(i - 1) & 1][m];
                }
            }
            if (i > 0 && u == BARU) {
                bar();
#pragma unroll
                for (int m = 0; m < NA; ++m) in[m] = rd[(((i - 1) & 1) * NA + m) * 64];
            }
            SB();
        }
        }
#pragma unroll
    for (int m = 0; m < NA; ++m) res[m] = acc[1][m] + in[m];
}

DI void aux_load(u32x4 (&ax)[4][5], u32x4 (&ay)[4][5], const Params& p, int aj, int w, int lane) {
    const int n = lane & 15, q = lane >> 4;
    const rsrc_t rw1 = mkrs(p.w1, D * F * 2);
#pragma unroll
    for (int i = 0; i < 4; ++i)
#pragma unroll
        for (int kk = 0; kk < 5; ++kk) {
            const unsigned d = 160 * w + 32 * kk + 8 * (n >> 2) + (n & 3), fo = (128 * aj + 32 * ((w - 1 - i) & 3) + 8 * q) * 2u;
            ax[i][kk] = ld16(rw1, d * (F * 2) + fo);
            ay[i][kk] = ld16(rw1, (d + 4) * (F * 2) + fo);
        }
}
DI void phase_aux(const Params& p, const u32x4 (&Q)[5][5], const u32x4 (&ax)[4][5], const u32x4 (&ay)[4][5], int ct, int aj, u64 ep, char* smem) {
    IDS;
    const rsrc_t rw2 = mkrs(p.w2, D * F * 2), rws = mkrs(p.ws, WS_USED);
    u32x4 I0, I1;
#pragma unroll
    for (int dd = 0; dd < 4; ++dd) {
        const int k = 8 * q + 2 * dd;
        I0[dd] = (k == n ? 0x3f80u : 0u) | (k + 1 == n ? 0x3f800000u : 0u);
        I1[dd] = (k == n + 16 ? 0x3f80u : 0u) | (k + 1 == n + 16 ? 0x3f800000u : 0u);
    }
    const f32x4 z = {0.f, 0.f, 0.f, 0.f};
    u32x4 a2[4][2][5];
    {
        f32x4 r[10];
        ring<5, 10, 2, 70>(r, (f32x4*)smem, w, lane, [&](int i, int kk, f32x4 (&a)[10]) {
            const f32x4 x0 = mfma(ax[i][kk], I0, z), x1 = mfma(ax[i][kk], I1, z), y0 = mfma(ay[i][kk], I0, z), y1 = mfma(ay[i][kk], I1, z);
            const u32x4 b0 = {pk(x0[0], x0[1]), pk(x0[2], x0[3]), pk(y0[0], y0[1]), pk(y0[2], y0[3])};
            const u32x4 b1 = {pk(x1[0], x1[1]), pk(x1[2], x1[3]), pk(y1[0], y1[1]), pk(y1[2], y1[3])};
#pragma unroll
            for (int mt = 0; mt < 5; ++mt) { a[mt] = mfma(Q[mt][kk], b0, a[mt]); a[5 + mt] = mfma(Q[mt][kk], b1, a[5 + mt]); }
#pragma unroll
            for (int e = 0; e < 2; ++e)
                a2[i][e][kk] = ld16(rw2, (128 * aj + 32 * ((w - 1 - i) & 3) + 16 * e + n) * 1280u + (160 * w + 32 * kk + 8 * q) * 2u);
        });

#pragma unroll
        for (int e = 0; e < 2; ++e)
#pragma unroll
            for (int mt = 0; mt < 5; ++mt) {
                f32x4 v = r[e * 5 + mt] * AMP;
                if (mt == 0 && ct == 0 && q == 0) { v[0] = 0.f; v[1] *= AMP0 / AMP; }
                const unsigned ft = 8 * aj + 2 * w + e, T = 5 * ct + mt;
                cst8(rws, O_W1F + ((ft * 20 + (T >> 1)) * 64 + lane) * 16u + (T & 1) * 8u, (u32x2){pk(v[0], v[1]), pk(v[2], v[3])});
            }
    }
    publish(p.ws, PH_A1, ct * 12 + aj, ep);

    {
        f32x4 r[10];
        ring<5, 10, 2, 73>(r, (f32x4*)smem, w, lane, [&](int i, int kk, f32x4 (&a)[10]) {
#pragma unroll
            for (int mt = 0; mt < 5; ++mt) { a[mt] = mfma(a2[i][0][kk], Q[mt][kk], a[mt]); a[5 + mt] = mfma(a2[i][1][kk], Q[mt][kk], a[5 + mt]); }
#pragma unroll
            for (int mt = 0; mt < 5; ++mt) {
                const int T = 5 * ct + mt;
                if ((T >> 1) == 5 * w + kk) {
                    const u32x4 I = (T & 1) ? I1 : I0;
                    const f32x4 t0 = mfma(a2[i][0][kk], I, z), t1 = mfma(a2[i][1][kk], I, z);
                    cst16(rws, O_W2T + ((T * 48 + 4 * aj + ((w - 1 - i) & 3)) * 64 + lane) * 16u, (u32x4){pk(t0[0], t0[1]), pk(t0[2], t0[3]), pk(t1[0], t1[1]), pk(t1[2], t1[3])});
                }
            }
        });

#pragma unroll
        for (int mt = 0; mt < 5; ++mt) {
            const float am = (mt == 0 && ct == 0 && n < 2) ? AMP0 : AMP;
            const f32x4 v0 = r[mt] * am, v1 = r[5 + mt] * am;
            cst16(rws, O_W2F + (((5 * ct + mt) * 48 + 4 * aj + w) * 64 + lane) * 16u, (u32x4){pk(v0[0], v0[1]), pk(v0[2], v0[3]), pk(v1[0], v1[1]), pk(v1[2], v1[3])});
        }
    }
    publish(p.ws, PH_A2, ct * 12 + aj, ep);
}

template <int L>
DI void phase_c(const Params& p, int g, u64 ep, char* smem) {
    constexpr unsigned o_in = L ? O_XH2 : O_XH0, o_st = L ? O_ST2 : O_ST0, o_out = L ? O_XH3 : O_XH1;
    constexpr int SO = L ? 28 : 8;
    IDS;
    const rsrc_t rws = mkrs(p.ws, WS_USED);
    const int c = w >> 1, kh = w & 1;
    u32x4 B1r[2], B1i[2], A3[4][4], A6[2][2];
    f32x2 twc[4][2], tws[4][2];
    {
        const float ka = (float)(16 * kh + n);
#pragma unroll
        for (int ks = 0; ks < 2; ++ks)
#pragma unroll
            for (int m = 0; m < 4; ++m) {
                const float r = (float)(16 * ks + 4 * q + m) * (1.f / 32.f) * ka;
                const float cs = vcos(r), sn = vsin(r);
                B1r[ks][m] = pk(cs, sn); B1i[ks][m] = pk(-sn, cs);
            }
#pragma unroll
        for (int bt = 0; bt < 4; ++bt)
#pragma unroll
            for (int r = 0; r < 4; ++r) {
                const float a = (float)(16 * bt + 4 * q + r) * ka * (1.f / 2048.f);
                twc[bt][r >> 1][r & 1] = vcos(a); tws[bt][r >> 1][r & 1] = vsin(a);
            }
#pragma unroll
        for (int bt = 0; bt < 4; ++bt)
#pragma unroll
            for (int m = 0; m < 4; ++m) {
                const float a = (float)n * (float)(16 * bt + 4 * q + m) * (1.f / 64.f);
                const float cs = vcos(a), sn = vsin(a);
#pragma unroll
                for (int mt = 0; mt < 4; ++mt) {
                    const int k4 = (mt * m) & 3;
                    A3[mt][bt][m] = k4 == 0 ? pk(cs, sn) : k4 == 1 ? pk(-sn, cs) : k4 == 2 ? pk(-cs, -sn) : pk(sn, -cs);
                }
            }
        const float rio = (float)(n & 1) * -0.25f;
#pragma unroll
        for (int rt = 0; rt < 2; ++rt)
#pragma unroll
            for (int ks = 0; ks < 2; ++ks)
#pragma unroll
                for (int m = 0; m < 4; ++m) {
                    const float a = (float)(16 * (w >> 1) + 8 * rt + (n >> 1)) * (float)(16 * ks + 4 * q + m) * (1.f / 32.f) + rio;
                    A6[rt][ks][m] = pk(vcos(a) * (1.f / 2048.f), vcos(a + 0.25f) * (1.f / 2048.f));
                }
#pragma unroll
        for (int i = 0; i < 4; ++i) {
            if (i < 2) { PIN(B1r[i]); PIN(B1i[i]); PIN(A6[i][0]); PIN(A6[i][1]); }
            PIN(A3[i][0]); PIN(A3[i][1]); PIN(A3[i][2]); PIN(A3[i][3]);
            PIN(twc[i][0]); PIN(twc[i][1]); PIN(tws[i][0]); PIN(tws[i][1]);
        }
    }
    SB();

    wait_flags(p.ws, L ? PH_G2 : PH_P0, tid, ep);
    bar();

    u32x4 xin[4], st[8], wf[4];
    {
        const int h = q & 1, hi = q >> 1;
#pragma unroll
        for (int cp = 0; cp < 8; ++cp) st[cp] = cld16(rws, o_st + ((cp * 4 + w) * 64 + lane) * 16u);
#pragma unroll
        for (int ii = 0; ii < 4; ++ii) xin[ii] = cld16(rws, o_in + g * GS + (2 * (8 * w + 4 * hi + ii) + h) * 256u + n * 16u);
#pragma unroll
        for (int mt = 0; mt < 4; ++mt) wf[mt] = cld16(rws, O_WFQ + ((2 * g + c) * 512 + (kh * 4 + mt) * 64 + lane) * 16u);
    }
    SB();
    u32x4* FR = (u32x4*)smem;
    u32x4* RAW = (u32x4*)(smem + 16384);
    {
        const int h = q & 1, hi = q >> 1;
        float rs[4][2];
        const unsigned sel0 = (unsigned)fresh_s(0x00003f80), sel1 = (unsigned)fresh_s(0x3f800000);
#pragma unroll
        for (int ii = 0; ii < 4; ++ii)
#pragma unroll
            for (int hf = 0; hf < 2; ++hf) {
                float s = 0.f;
#pragma unroll
                for (int cp = 0; cp < 8; ++cp) s = dot2(st[cp][ii], hf ? sel1 : sel0, s);
                rs[ii][hf] = __builtin_amdgcn_rsqf(__builtin_fmaf(s, 1.f / 640.f, 1e-5f));
            }
#pragma unroll
        for (int ii = 0; ii < 4; ++ii) RAW[((8 * w + 4 * hi + ii) * 2 + h) * 16 + n] = xin[ii];
        const int aq = 2 * w + hi;
#pragma unroll
        for (int hf = 0; hf < 2; ++hf)
#pragma unroll
            for (int cc = 0; cc < 2; ++cc) {
                u32x4 o;
#pragma unroll
                for (int ii = 0; ii < 4; ++ii) {
                    const unsigned u = xin[ii][hf * 2 + cc];
                    float re = blo(u) * rs[ii][hf];
                    if (cc == 0 && g == 0) re = 0.f;
                    o[ii] = pk(re, bhi(u) * rs[ii][hf]);
                }
                FR[((cc * 4 + 2 * h + hf) * 2 + (aq >> 2)) * 64 + (aq & 3) * 16 + n] = o;
            }
    }
    bar();

    f32x4 tr[4], ti[4];
    {
        u32x4 a[4][2];
#pragma unroll
        for (int bt = 0; bt < 4; ++bt) { a[bt][0] = FR[((c * 4 + bt) * 2 + 0) * 64 + lane]; a[bt][1] = FR[((c * 4 + bt) * 2 + 1) * 64 + lane]; }
        SB();
#pragma unroll
        for (int bt = 0; bt < 4; ++bt) {
            const f32x4 z = {0.f, 0.f, 0.f, 0.f};
            tr[bt] = mfma(a[bt][1], B1r[1], mfma(a[bt][0], B1r[0], z));
            ti[bt] = mfma(a[bt][1], B1i[1], mfma(a[bt][0], B1i[0], z));
        }
    }
    bar();
    u32x4 P[4], Pn[4];
#pragma unroll
    for (int bt = 0; bt < 4; ++bt)
#pragma unroll
        for (int r2 = 0; r2 < 2; ++r2) {
            const f32x2 a = {tr[bt][2 * r2], tr[bt][2 * r2 + 1]}, b = {ti[bt][2 * r2], ti[bt][2 * r2 + 1]};
            const f32x2 xr = a * twc[bt][r2] + b * tws[bt][r2], xi = b * twc[bt][r2] - a * tws[bt][r2];
            P[bt][2 * r2] = pk(xr[0], xi[0]); P[bt][2 * r2 + 1] = pk(xr[1], xi[1]);
            Pn[bt][2 * r2] = pk(xi[0], -xr[0]); Pn[bt][2 * r2 + 1] = pk(xi[1], -xr[1]);
        }
    u32x4 R[4], Rn[4];
#pragma unroll
    for (int mt = 0; mt < 4; ++mt) {
        f32x4 zr = {0.f, 0.f, 0.f, 0.f}, zi = {0.f, 0.f, 0.f, 0.f};
#pragma unroll
        for (int bt = 0; bt < 4; ++bt) { zr = mfma(A3[mt][bt], P[bt], zr); zi = mfma(A3[mt][bt], Pn[bt], zi); }
        unsigned pr[4], pi[4];
#pragma unroll
        for (int r = 0; r < 4; ++r) {
            const float fr = blo(wf[mt][r]), fi = bhi(wf[mt][r]);
            const float yr = zr[r] * fr - zi[r] * fi, yi = zr[r] * fi + zi[r] * fr;
            pr[r] = pk(yr, -yi); pi[r] = pk(yi, yr);
        }
        R[mt] = (u32x4){pr[0], pr[1], pr[2], pr[3]}; Rn[mt] = (u32x4){pi[0], pi[1], pi[2], pi[3]};
    }

    unsigned* TR = (unsigned*)smem;
#pragma unroll
    for (int bt = 0; bt < 4; ++bt) {
        f32x4 ur = {0.f, 0.f, 0.f, 0.f}, ui = {0.f, 0.f, 0.f, 0.f};
#pragma unroll
        for (int kt = 0; kt < 4; ++kt) { ur = mfma(A3[bt][kt], R[kt], ur); ui = mfma(A3[bt][kt], Rn[kt], ui); }
#pragma unroll
        for (int r2 = 0; r2 < 2; ++r2) {
            const f32x2 a = {ur[2 * r2], ur[2 * r2 + 1]}, b = {ui[2 * r2], ui[2 * r2 + 1]};
            const f32x2 xr = a * twc[bt][r2] - b * tws[bt][r2], xi = b * twc[bt][r2] + a * tws[bt][r2];
#pragma unroll
            for (int e = 0; e < 2; ++e) {
                const int r = 2 * r2 + e;
                const int b_ = 16 * bt + 4 * q + r;
                const int kq = 4 * kh + (n >> 2), f = (2 * q + r2) & 7;
                TR[c * 2048 + b_ * 32 + ((kq ^ f) << 2) + (n & 3)] = pk(xr[e], xi[e]);
            }
        }
    }
    bar();

    {
        const int h = w & 1, ah = w >> 1;
        const u32x4* TRv = (const u32x4*)smem;
        u32x4 bq[2][2][2], raw[2][2];
#pragma unroll
        for (int hf = 0; hf < 2; ++hf)
#pragma unroll
            for (int cc = 0; cc < 2; ++cc) {
                const int b_ = 16 * (2 * h + hf) + n, f = (n >> 1) & 7;
                bq[hf][cc][0] = TRv[cc * 512 + b_ * 8 + ((0 + q) ^ f)]; bq[hf][cc][1] = TRv[cc * 512 + b_ * 8 + ((4 + q) ^ f)];
            }
#pragma unroll
        for (int rt = 0; rt < 2; ++rt)
#pragma unroll
            for (int e = 0; e < 2; ++e) raw[rt][e] = RAW[((16 * ah + 8 * rt + 2 * q + e) * 2 + h) * 16 + n];
        SB();
        u32x4 o[2][2];
#pragma unroll
        for (int rt = 0; rt < 2; ++rt) {
#pragma unroll
            for (int hf = 0; hf < 2; ++hf)
#pragma unroll
                for (int cc = 0; cc < 2; ++cc) {
                    const unsigned r0 = raw[rt][0][hf * 2 + cc], r1 = raw[rt][1][hf * 2 + cc];
                    f32x4 y = {0.f, 0.f, 0.f, 0.f};
                    y = mfma(A6[rt][0], bq[hf][cc][0], y); y = mfma(A6[rt][1], bq[hf][cc][1], y);
                    if (cc == 0 && g == 0) { y[0] = 0.f; y[2] = 0.f; }
                    o[rt][0][hf * 2 + cc] = pk(y[0] + blo(r0), y[1] + bhi(r0));
                    o[rt][1][hf * 2 + cc] = pk(y[2] + blo(r1), y[3] + bhi(r1));
                }
#pragma unroll
            for (int e = 0; e < 2; ++e)
                cst16(rws, o_out + g * GS + (2 * (16 * ah + 8 * rt + 2 * q + e) + h) * 256u + n * 16u, o[rt][e]);
        }
    }

    publish(p.ws, L ? PH_C2 : PH_C1, g, ep);

}

template <int L>
DI void phase_g1(const Params& p, int tt, int fs, u32x4 (&W)[5][12], unsigned wo, u64 ep, char* smem) {
    constexpr unsigned o_in = L ? O_XH3 : O_XH1, o_hf = L ? O_HF2 : O_HF1;
    constexpr int SO = L ? 36 : 17;
    IDS;
    const rsrc_t rws = mkrs(p.ws, WS_USED), rb1 = mkrs(p.b1, F * 2);
    {
        const int e = w * 40 + (lane < 40 ? lane : 0);
        wait_flags(p.ws, L ? PH_C2 : PH_C1, e, ep);
    }
    bar();

    const int blk = 4 * tt + w;
    u32x4 l[20][2];
#pragma unroll
    for (int kk = 0; kk < 20; ++kk)
#pragma unroll
        for (int e = 0; e < 2; ++e) l[kk][e] = cld16(rws, o_in + (8 * kk + 4 * e + q) * GS + blk * 256u + n * 16u);
    u32x2 bb[6];
#pragma unroll
    for (int mt = 0; mt < 6; ++mt) bb[mt] = ld8(rb1, (96 * fs + 16 * mt + 4 * q) * 2u);
    SB();
    const u32x4* WL = (const u32x4*)smem + lane;
    f32x4 acc[6][2], gr0 = {0.f, 0.f, 0.f, 0.f}, gr1 = {0.f, 0.f, 0.f, 0.f};
#pragma unroll
    for (int mt = 0; mt < 6; ++mt) { acc[mt][0] = (f32x4){0.f, 0.f, 0.f, 0.f}; acc[mt][1] = (f32x4){0.f, 0.f, 0.f, 0.f}; }
    u32x4 A[2][6];
#pragma unroll
    for (int mt = 0; mt < 6; ++mt) A[0][mt] = WL[(mt * 20) * 64];
    u32x4 f0 = {l[0][0][0], l[0][0][1], l[0][1][0], l[0][1][1]}, f1 = {l[0][0][2], l[0][0][3], l[0][1][2], l[0][1][3]};
    if (q == 0) { f0[0] &= 0xffff0000u; f1[0] &= 0xffff0000u; }
    SB();
#pragma unroll
    for (int kk = 0; kk < 20; ++kk) {
        u32x4 g0, g1;
#pragma unroll
        for (int mt = 0; mt < 6; ++mt) {
            acc[mt][0] = mfma(A[kk & 1][mt], f0, acc[mt][0]);
            acc[mt][1] = mfma(A[kk & 1][mt], f1, acc[mt][1]);
            if (kk < 19) A[(kk + 1) & 1][mt] = WL[(mt * 20 + kk + 1) * 64];
            if (mt == 0) gr0 = mfma(f0, f0, gr0);
            if (mt == 1) gr1 = mfma(f1, f1, gr1);
            if (mt == 2 && kk < 19) {
                g0 = (u32x4){l[kk + 1][0][0], l[kk + 1][0][1], l[kk + 1][1][0], l[kk + 1][1][1]};
                g1 = (u32x4){l[kk + 1][0][2], l[kk + 1][0][3], l[kk + 1][1][2], l[kk + 1][1][3]};
            }
            if (kk >= 5 && (mt == 3 || mt == 4)) {
                const int e = 2 * (kk - 5) + (mt - 3), u = 6 + e / 5, m5 = e % 5;
                W[m5][u] = cld16(rws, wo + (m5 * 48 + u) * 1024u);
            }
            SB();
        }
        if (kk < 19) { f0 = g0; f1 = g1; }
        }

    float rs[2];
    {
        const int r = n & 3;
        const float s0 = r == 0 ? gr0[0] : r == 1 ? gr0[1] : r == 2 ? gr0[2] : gr0[3];
        const float s1 = r == 0 ? gr1[0] : r == 1 ? gr1[1] : r == 2 ? gr1[2] : gr1[3];
        const bool dg = q == (n >> 2);
        rs[0] = __builtin_amdgcn_rsqf(__builtin_fmaf(sumq(dg ? s0 : 0.f), 1.f / 640.f, 1e-5f));
        rs[1] = __builtin_amdgcn_rsqf(__builtin_fmaf(sumq(dg ? s1 : 0.f), 1.f / 640.f, 1e-5f));
    }
    constexpr float C0 = -2.3022081985f, C1 = -0.1029432397f;
#pragma unroll
    for (int hf = 0; hf < 2; ++hf)
#pragma unroll
        for (int u = 0; u < 3; ++u) {
            u32x4 o;
#pragma unroll
            for (int e = 0; e < 2; ++e) {
                const int mt = 2 * u + e;
                float hv[4];
#pragma unroll
                for (int r = 0; r < 4; ++r) {
                    const float bv = (r & 1) ? bhi(bb[mt][r >> 1]) : blo(bb[mt][r >> 1]);
                    const float x = __builtin_fmaf(acc[mt][hf][r], rs[hf], bv);
                    const float ex = __builtin_amdgcn_exp2f(x * __builtin_fmaf(x * x, C1, C0));
                    hv[r] = x * __builtin_amdgcn_rcpf(1.f + ex);
                }
                o[2 * e] = pk(hv[0], hv[1]); o[2 * e + 1] = pk(hv[2], hv[3]);
            }
            cst16(rws, o_hf + (((2 * blk + hf) * 48 + 3 * fs + u) * 64 + lane) * 16u, o);
        }

    publish(p.ws, L ? PH_G1B : PH_G1A, tt * 16 + fs, ep);

}

DI void phase_g2(const Params& p, int id, int rb, int ct, const u32x4 (&W)[5][12], u64 ep, char* scr) {
    IDS;
    const rsrc_t rws = mkrs(p.ws, WS_USED);
    wait_flags(p.ws, PH_G1A, (rb >> 1) * 16 + 4 * w + (lane & 3), ep);

    u32x4 hf[2][12];
    const unsigned ho = O_HF1 + ((4 * rb * 48 + 12 * w) * 64 + lane) * 16u;
#pragma unroll
    for (int i = 0; i < 2; ++i)
#pragma unroll
        for (int u = 0; u < 12; ++u) hf[i][u] = cld16(rws, ho + (((w - 1 - i) & 3) * 48 + u) * 1024u);
    u32x2 res[5]; f32x4 bv[5];
#pragma unroll
    for (int mt = 0; mt < 5; ++mt) {
        res[mt] = cld8(rws, O_XH1 + (20 * ct + 4 * mt + q) * GS + (2 * rb + (w >> 1)) * 256u + n * 16u + (w & 1) * 8u);
        bv[mt] = __builtin_bit_cast(f32x4, ld16(rws, O_B2P + id * 512u + (16 * mt + 4 * q) * 4u));
    }
    SB();
    f32x4 acc[5];
    ring<12, 5, 5, 54>(acc, (f32x4*)scr, w, lane, [&](int i, int u, f32x4 (&a)[5]) {
#pragma unroll
        for (int mt = 0; mt < 5; ++mt) a[mt] = mfma(W[mt][u], hf[i & 1][u], a[mt]);
        if (i < 2) hf[i & 1][u] = cld16(rws, ho + (((w - 3 - i) & 3) * 48 + u) * 1024u);
    });

#pragma unroll
    for (int mt = 0; mt < 5; ++mt) {
        acc[mt] += bv[mt];
        acc[mt][0] += blo(res[mt][0]); acc[mt][1] += bhi(res[mt][0]); acc[mt][2] += blo(res[mt][1]); acc[mt][3] += bhi(res[mt][1]);
    }
    store_xh(acc, rws, O_XH2, O_ST2, rb, ct, w, lane, scr + 5 * 1024);

    publish(p.ws, PH_G2, rb * 8 + ct, ep);

}

DI void phase_g4(const Params& p, int rb, int ct, const u32x4 (&W)[5][12], u64 ep, char* smem) {
    IDS;
    const rsrc_t rws = mkrs(p.ws, WS_USED), rb2 = mkrs(p.b2, D * 2), rout = mkrs(p.out, S * D * 2);
    f32x4* xp = (f32x4*)smem;
    u32x4 hf[2][12];
    const unsigned ho = O_HF2 + ((4 * rb * 48 + 12 * w) * 64 + lane) * 16u;
    {
        u32x4 xg[2][5][2];
#pragma unroll
        for (int b = 0; b < 2; ++b)
#pragma unroll
            for (int kk = 0; kk < 5; ++kk)
#pragma unroll
                for (int e = 0; e < 2; ++e)
                    xg[b][kk][e] = cld16(rws, O_XH3 + (40 * w + 8 * kk + 4 * e + q) * GS + (2 * rb + b) * 256u + n * 16u);
        SB();
        u32x4 xf[4][5];
#pragma unroll
        for (int mt = 0; mt < 5; ++mt) {
            if (mt == 3) {

                wait_flags(p.ws, PH_G1B, (rb >> 1) * 16 + 4 * w + (lane & 3), ep);

#pragma unroll
                for (int i = 0; i < 2; ++i)
#pragma unroll
                    for (int u = 0; u < 12; ++u) hf[i][u] = cld16(rws, ho + (((w - 1 - i) & 3) * 48 + u) * 1024u);
                SB();
            }
            u32x4 Q[5];
            const float df = (float)(80 * ct + 16 * mt + n);
#pragma unroll
            for (int kk = 0; kk < 5; ++kk)
#pragma unroll
                for (int hh = 0; hh < 2; ++hh)
#pragma unroll
                    for (int pr = 0; pr < 2; ++pr) {
                        const int c = 160 * w + 32 * kk + 16 * hh + 4 * q + 2 * pr;
                        const float t = (float)(c >> 1) * df;
                        const float k = floorf(t * (1.f / 640.f));
                        const float r = __builtin_fmaf(-k, 640.f, t) * (1.f / 640.f);
                        float v0 = vcos(r) * AMP, v1 = vcos(r + 0.25f) * AMP;
                        if (kk == 0 && hh == 0 && pr == 0 && c == 0) { v0 = AMP0; v1 = (n & 1) ? -AMP0 : AMP0; }
                        Q[kk][2 * hh + pr] = pk(v0, v1);
                    }
            if (mt == 0) {
#pragma unroll
                for (int t = 0; t < 4; ++t)
#pragma unroll
                    for (int kk = 0; kk < 5; ++kk) {
                        const int b = t >> 1, o = 2 * (t & 1);
                        xf[t][kk] = (u32x4){xg[b][kk][0][o], xg[b][kk][0][o + 1], xg[b][kk][1][o], xg[b][kk][1][o + 1]};
                    }
            }
            f32x4 a[4];
#pragma unroll
            for (int t = 0; t < 4; ++t) a[t] = (f32x4){0.f, 0.f, 0.f, 0.f};
#pragma unroll
            for (int kk = 0; kk < 5; ++kk)
#pragma unroll
                for (int t = 0; t < 4; ++t) a[t] = mfma(Q[kk], xf[t][kk], a[t]);
#pragma unroll
            for (int t = 0; t < 4; ++t) xp[((w * 4 + t) * 5 + mt) * 64 + lane] = a[t];
            SB();
        }
    }
    u32x2 bb[5];
#pragma unroll
    for (int mt = 0; mt < 5; ++mt) bb[mt] = ld8(rb2, (80 * ct + 16 * mt + 4 * q) * 2u);
    SB();

    f32x4 acc[5];
    ring<12, 5, 5, 64>(acc, (f32x4*)(smem + LDS_SCR), w, lane, [&](int i, int u, f32x4 (&a)[5]) {
        if (u == 0) {
#pragma unroll
            for (int mt = 0; mt < 5; ++mt) a[mt] = xp[((w * 4 + ((w - 1 - i) & 3)) * 5 + mt) * 64 + lane];
        }
#pragma unroll
        for (int mt = 0; mt < 5; ++mt) a[mt] = mfma(W[mt][u], hf[i & 1][u], a[mt]);
        if (i < 2) hf[i & 1][u] = cld16(rws, ho + (((w - 3 - i) & 3) * 48 + u) * 1024u);
    });

#pragma unroll
    for (int mt = 0; mt < 5; ++mt) {
        const unsigned d0 = pk(acc[mt][0] + blo(bb[mt][0]), acc[mt][1] + bhi(bb[mt][0]));
        const unsigned d1 = pk(acc[mt][2] + blo(bb[mt][1]), acc[mt][3] + bhi(bb[mt][1]));
        st8(rout, (64 * rb + 16 * w + n) * 1280u + (80 * ct + 16 * mt + 4 * q) * 2u, (u32x2){d0, d1});
    }

}

__global__ void __launch_bounds__(256, 1) k_all(Params p) {
    __shared__ __attribute__((aligned(16))) char smem[163840];
    char* scr = smem + LDS_SCR;

#define MAP const int id = fresh_s(blockIdx.x); const Map mp = get_map(id)
    GAS u64* epw = (GAS u64*)(p.ws + O_EPO);
    const u64 ep_in = __hip_atomic_load(epw, __ATOMIC_RELAXED, __HIP_MEMORY_SCOPE_AGENT);

    u32x4 Q[5][5], ax[4][5], ay[4][5];
    {
        MAP; IDS;
        const rsrc_t rws = mkrs(p.ws, WS_USED);
        const rsrc_t rwr = mkrs(p.wr, S * 321 * 2), rwi = mkrs(p.wi, S * 321 * 2);
        const int ks0 = 16 * (mp.rb >> 4) + 32 * (16 * ((mp.rb >> 2) & 3) + 4 * (mp.rb & 3));
        unsigned fr[2][4], fi[2][4], fo[2];
        {
            const int ql = tid & 15, pc = 40 * mp.ct + 2 * (tid >> 4);
            fo[0] = O_WFQ + (pc * 512 + 16 * mp.rb + ql) * 16u;
#pragma unroll
            for (int jj = 0; jj < 4; ++jj) {
                const unsigned o1 = ((ks0 + ql + 32 * jj) * 321 + pc) * 2u;
                fr[0][jj] = ld4(rwr, o1); fi[0][jj] = ld4(rwi, o1);
            }
        }
        if (w == 0) {
            const int ql = lane & 15, pc = 40 * mp.ct + 32 + 2 * (lane >> 4);
            fo[1] = O_WFQ + (pc * 512 + 16 * mp.rb + ql) * 16u;
#pragma unroll
            for (int jj = 0; jj < 4; ++jj) {
                const unsigned o1 = ((ks0 + ql + 32 * jj) * 321 + pc) * 2u;
                fr[1][jj] = ld4(rwr, o1); fi[1][jj] = ld4(rwi, o1);
            }
        } else if (w == 1 && mp.ct == 0) {
            const int ql = lane >> 2, jj = lane & 3, ks = ks0 + ql + 32 * jj, kn = (2048 - ks) & 2047;
            fo[1] = O_WFQ + (16 * mp.rb + ql) * 16u + jj * 4u;
            fr[1][0] = ld2(rwr, (ks * 321 + 320) * 2u); fr[1][1] = ld2(rwr, (kn * 321 + 320) * 2u);
            fi[1][0] = ld2(rwi, (ks * 321 + 320) * 2u); fi[1][1] = ld2(rwi, (kn * 321 + 320) * 2u);
        }
        SB();

        const rsrc_t rx = mkrs(p.x, S * D * 2);
        u32x4 xf[4][5];
#pragma unroll
        for (int i = 0; i < 4; ++i)
#pragma unroll
            for (int kk = 0; kk < 5; ++kk)
                xf[i][kk] = ld16(rx, (64 * mp.rb + 16 * ((w - 1 - i) & 3) + n) * 1280u + (160 * w + 32 * kk + 8 * q) * 2u);
        SB();

        gen_q(Q, mp.ct, w, lane);
#pragma unroll
        for (int mt = 0; mt < 5; ++mt)
#pragma unroll
            for (int kk = 0; kk < 5; ++kk) PIN(Q[mt][kk]);
        SB();

        {
            u32x4 o0, o1;
#pragma unroll
            for (int jj = 0; jj < 4; ++jj) {
                o0[jj] = __builtin_amdgcn_perm(fi[0][jj], fr[0][jj], 0x05040100u);
                o1[jj] = __builtin_amdgcn_perm(fi[0][jj], fr[0][jj], 0x07060302u);
            }
            cst16(rws, (mp.ct == 0 && tid < 16) ? 0xfffffff0u : fo[0], o0);
            cst16(rws, fo[0] + 8192, o1);
        }
        if (w == 0) {
            u32x4 o0, o1;
#pragma unroll
            for (int jj = 0; jj < 4; ++jj) {
                o0[jj] = __builtin_amdgcn_perm(fi[1][jj], fr[1][jj], 0x05040100u);
                o1[jj] = __builtin_amdgcn_perm(fi[1][jj], fr[1][jj], 0x07060302u);
            }
            cst16(rws, fo[1], o0);
            cst16(rws, fo[1] + 8192, o1);
        } else if (w == 1 && mp.ct == 0) {
            cst4(rws, fo[1], pk((blo(fr[1][0]) + blo(fr[1][1])) * 0.5f, (blo(fi[1][0]) - blo(fi[1][1])) * 0.5f));
        }
        SB();

        f32x4 acc[5];
        ring<5, 5, 2, 47>(acc, (f32x4*)scr, w, lane, [&](int i, int kk, f32x4 (&a)[5]) {
#pragma unroll
            for (int mt = 0; mt < 5; ++mt) a[mt] = mfma(Q[mt][kk], xf[i][kk], a[mt]);
        });

#pragma unroll
        for (int mt = 0; mt < 5; ++mt) {
            acc[mt] *= AMP;
            if (mt == 0 && mp.ct == 0 && q == 0) { acc[0][0] *= AMP0 / AMP; acc[0][1] *= AMP0 / AMP; }
        }
        store_xh(acc, rws, O_XH0, O_ST0, mp.rb, mp.ct, w, lane, scr + 5 * 1024);

    }
    const u64 ep = ((u64)__builtin_amdgcn_readfirstlane((unsigned)(ep_in >> 32)) << 32 | __builtin_amdgcn_readfirstlane((unsigned)ep_in)) + 1;
    {
        MAP; IDS;
        const rsrc_t rws = mkrs(p.ws, WS_USED);
        publish(p.ws, PH_P0, mp.rb * 8 + mp.ct, ep);

        if (!mp.isC) aux_load(ax, ay, p, mp.aj, w, lane);
        SB();
        const rsrc_t rb2 = mkrs(p.b2, D * 2);
        u32x4 b2f[5];
#pragma unroll
        for (int kk = 0; kk < 5; ++kk) b2f[kk] = ld16(rb2, (160 * w + 32 * kk + 8 * q) * 2u);
        f32x4* pb = (f32x4*)scr;
#pragma unroll
        for (int mt = 0; mt < 5; ++mt) {
            f32x4 a = {0.f, 0.f, 0.f, 0.f};
#pragma unroll
            for (int kk = 0; kk < 5; ++kk) a = mfma(Q[mt][kk], b2f[kk], a);
            if (n == 0) pb[w * 20 + mt * 4 + q] = a;
        }
        bar();
        if (tid < 20) {
            f32x4 s = (pb[lane] + pb[20 + lane] + pb[40 + lane] + pb[60 + lane]) * AMP;
            if (lane == 0 && mp.ct == 0) { s[0] *= AMP0 / AMP; s[1] *= AMP0 / AMP; }
            st16(rws, O_B2P + id * 512u + lane * 16u, __builtin_bit_cast(u32x4, s));
        }
    }

    {
        MAP;
        if (mp.isC) phase_c<0>(p, mp.g, ep, scr);
        else phase_aux(p, Q, ax, ay, mp.ct, mp.aj, ep, smem);
    }

    u32x4 W[5][12];
    {
        MAP; IDS;
        const rsrc_t rws = mkrs(p.ws, WS_USED);
        const int a0 = (96 * mp.m) >> 7, a1 = (96 * mp.m + 95) >> 7;
        wait_flags(p.ws, PH_A1, (lane & 7) * 12 + ((lane & 8) ? a1 : a0), ep);
        u32x4 t[30];
#pragma unroll
        for (int i = 0; i < 30; ++i) t[i] = cld16(rws, O_W1F + mp.m * 122880u + w * 30720u + i * 1024u + lane * 16u);
        SB();
        u32x4* dst = (u32x4*)(smem + w * 30720) + lane;
#pragma unroll
        for (int i = 0; i < 30; ++i) dst[i * 64] = t[i];
        SB();
        wait_flags(p.ws, PH_A2, mp.ct * 12 + 3 * w + (lane % 3), ep);
        const unsigned wo = O_W2F + ((5 * mp.ct * 48 + 12 * w) * 64 + lane) * 16u;
#pragma unroll
        for (int u = 0; u < 6; ++u)
#pragma unroll
            for (int mt = 0; mt < 5; ++mt) W[mt][u] = cld16(rws, wo + (mt * 48 + u) * 1024u);
        SB();

        phase_g1<0>(p, mp.tt, mp.m, W, wo, ep, smem);
    }
    { MAP; phase_g2(p, id, mp.rb, mp.ct, W, ep, scr); }

    { MAP; if (mp.isC) phase_c<1>(p, mp.g, ep, scr); }
    {
        MAP; IDS;
        const rsrc_t rws = mkrs(p.ws, WS_USED);
        const unsigned wo = O_W2T + ((5 * mp.ct * 48 + 12 * w) * 64 + lane) * 16u;
#pragma unroll
        for (int u = 0; u < 6; ++u)
#pragma unroll
            for (int mt = 0; mt < 5; ++mt) W[mt][u] = cld16(rws, wo + (mt * 48 + u) * 1024u);
        SB();

        phase_g1<1>(p, mp.tt, mp.m, W, wo, ep, smem);
    }
    { MAP; phase_g4(p, mp.rb, mp.ct, W, ep, smem); }
    if (blockIdx.x == 0 && threadIdx.x == 0) __hip_atomic_store(epw, ep, __ATOMIC_RELAXED, __HIP_MEMORY_SCOPE_AGENT);

}

extern "C" void kernel_launch(void* const* d_in, const int* in_sizes, int n_in, void* d_out, int out_size, void* d_ws, size_t ws_size,
                              hipStream_t stream) {
    Params p;
    p.x = (const uint16_t*)d_in[0]; p.wr = (const uint16_t*)d_in[1]; p.wi = (const uint16_t*)d_in[2];
    p.w1 = (const uint16_t*)d_in[4]; p.b1 = (const uint16_t*)d_in[5]; p.w2 = (const uint16_t*)d_in[6]; p.b2 = (const uint16_t*)d_in[7];
    p.out = (uint16_t*)d_out; p.ws = (char*)d_ws;
    k_all<<<256, 256, 0, stream>>>(p);
}
```

```cpp
#include <hip/hip_runtime.h>
#include <stdint.h>

#pragma clang diagnostic ignored "-Winline-asm"
typedef float f32x4 __attribute__((ext_vector_type(4)));
typedef float f32x2 __attribute__((ext_vector_type(2)));
typedef unsigned u32x4 __attribute__((ext_vector_type(4)));
typedef unsigned u32x2 __attribute__((ext_vector_type(2)));
typedef __bf16 bf16x8 __attribute__((ext_vector_type(8)));
typedef __bf16 bf16x2 __attribute__((ext_vector_type(2)));
typedef __amdgpu_buffer_rsrc_t rsrc_t;
typedef unsigned long long u64;
#define DI __device__ __forceinline__
#define GAS __attribute__((address_space(1)))
#define LAS __attribute__((address_space(3)))

constexpr int S = 2048, D = 640, F = 1536;
#define GSPAD 0
constexpr unsigned GS = 16384u + GSPAD;
constexpr unsigned XH_BYTES = 160u * GS;
constexpr unsigned O_XH0 = 0, O_XH1 = XH_BYTES, O_XH2 = 2 * XH_BYTES, O_XH3 = 3 * XH_BYTES;
constexpr unsigned O_HF1 = 0x0B00000, O_HF2 = 0x1100000;
constexpr unsigned O_W1F = 0x1700000;
constexpr unsigned O_W2F = 0x1900000;
constexpr unsigned O_W2T = 0x1B00000;
constexpr unsigned O_WFQ = 0x1D00000;
constexpr unsigned O_ST0 = 0x1F80000, O_ST2 = 0x1F90000;
constexpr unsigned O_FLG = 0x1FA0000;
constexpr unsigned O_EPO = 0x1FF0000;
constexpr unsigned O_QT = 0x2000000;
constexpr unsigned O_B2P = 0x20D0000;
constexpr unsigned WS_USED = 0x2100000;
constexpr unsigned LDS_SCR = 122880;
enum { PH_P0 = 0, PH_A1, PH_A2, PH_A3, PH_C1, PH_G1A, PH_G2, PH_C2, PH_G1B };

struct Params {
    const uint16_t* x; const uint16_t* wr; const uint16_t* wi; const uint16_t* w1; const uint16_t* b1;
    const uint16_t* w2; const uint16_t* b2; uint16_t* out; char* ws;
};

#define PIN(x) asm volatile("" : "+v"(x))

DI rsrc_t mkrs(const void* p, unsigned bytes) { return __builtin_amdgcn_make_buffer_rsrc((void*)p, 0, bytes, 0x00020000); }
DI u32x4 ld16(rsrc_t r, unsigned off) { return __builtin_amdgcn_raw_buffer_load_b128(r, off, 0, 0); }
DI u32x2 ld8(rsrc_t r, unsigned off) { return __builtin_amdgcn_raw_buffer_load_b64(r, off, 0, 0); }
DI unsigned ld4(rsrc_t r, unsigned off) { return __builtin_amdgcn_raw_buffer_load_b32(r, off, 0, 0); }
DI unsigned ld2(rsrc_t r, unsigned off) { return __builtin_amdgcn_raw_buffer_load_b16(r, off, 0, 0); }
DI void st16(rsrc_t r, unsigned off, u32x4 v) { __builtin_amdgcn_raw_buffer_store_b128(v, r, off, 0, 0); }
DI void st8(rsrc_t r, unsigned off, u32x2 v) { __builtin_amdgcn_raw_buffer_store_b64(v, r, off, 0, 0); }
DI u32x4 cld16(rsrc_t r, unsigned off) { return __builtin_amdgcn_raw_buffer_load_b128(r, off, 0, 16); }
DI u32x2 cld8(rsrc_t r, unsigned off) { return __builtin_amdgcn_raw_buffer_load_b64(r, off, 0, 16); }
DI void cst16(rsrc_t r, unsigned off, u32x4 v) { __builtin_amdgcn_raw_buffer_store_b128(v, r, off, 0, 16); }
DI void cst8(rsrc_t r, unsigned off, u32x2 v) { __builtin_amdgcn_raw_buffer_store_b64(v, r, off, 0, 16); }
DI void cst4(rsrc_t r, unsigned off, unsigned v) { __builtin_amdgcn_raw_buffer_store_b32(v, r, off, 0, 16); }
DI void dma16(rsrc_t r, unsigned voff, unsigned soff, unsigned lds) {
    asm volatile("s_mov_b32 m0, %0\n\ts_nop 0\n\tbuffer_load_dwordx4 %1, %2, %3 offen lds" :: "s"(lds), "v"(voff), "s"(r), "s"(soff) : "m0", "memory");
}
typedef short s16x4 __attribute__((ext_vector_type(4)));
DI u32x2 trd(const char* a) { return __builtin_bit_cast(u32x2, __builtin_amdgcn_ds_read_tr16_b64_v4i16((LAS s16x4*)a)); }
DI f32x4 mfma(u32x4 a, u32x4 b, f32x4 c) {
    return __builtin_amdgcn_mfma_f32_16x16x32_bf16(__builtin_bit_cast(bf16x8, a), __builtin_bit_cast(bf16x8, b), c, 0, 0, 0);
}
DI unsigned pk(float lo, float hi) { bf16x2 v = __builtin_convertvector((f32x2){lo, hi}, bf16x2); return __builtin_bit_cast(unsigned, v); }
DI float blo(unsigned u) { return __uint_as_float(u << 16); }
DI float bhi(unsigned u) { return __uint_as_float(u & 0xffff0000u); }
DI float dot2(unsigned a, unsigned b, float c) { return __builtin_amdgcn_fdot2_f32_bf16(__builtin_bit_cast(bf16x2, a), __builtin_bit_cast(bf16x2, b), c, false); }
DI float vcos(float r) { return __builtin_amdgcn_cosf(r); }
DI float vsin(float r) { return __builtin_amdgcn_sinf(r); }
DI float fract(float r) { return __builtin_amdgcn_fractf(r); }
DI void bar() { asm volatile("s_waitcnt lgkmcnt(0)\n\ts_barrier" ::: "memory"); }
DI float sumq(float v) {
    auto a = __builtin_amdgcn_permlane16_swap(__float_as_uint(v), __float_as_uint(v), false, false);
    v = __uint_as_float(a[0]) + __uint_as_float(a[1]);
    auto b = __builtin_amdgcn_permlane32_swap(__float_as_uint(v), __float_as_uint(v), false, false);
    return __uint_as_float(b[0]) + __uint_as_float(b[1]);
}
#define SB() __builtin_amdgcn_sched_barrier(0)
DI int fresh_tid() { int t = threadIdx.x; asm volatile("" : "+v"(t)); return t; }
DI unsigned rfl(unsigned v) { return (unsigned)__builtin_amdgcn_readfirstlane((int)v); }
DI u64 rfl64(u64 v) { return (u64)rfl((unsigned)(v >> 32)) << 32 | (u64)rfl((unsigned)v); }
DI int fresh_s(int v) { asm volatile("" : "+s"(v)); return v; }
#define IDS const int tid = fresh_tid(), w = __builtin_amdgcn_readfirstlane(tid >> 6), lane = tid & 63, n = lane & 15, q = lane >> 4

DI void publish(char* ws, int ph, int idx, u64 ep) {
    asm volatile("s_waitcnt vmcnt(0)" ::: "memory");
    bar();
    if (threadIdx.x == 0) __hip_atomic_store((GAS u64*)(ws + O_FLG + (ph * 256 + idx) * 128), ep, __ATOMIC_RELAXED, __HIP_MEMORY_SCOPE_AGENT);
}
DI void wait_flags(char* ws, int ph, int idx, u64 ep) {
    GAS u64* f = (GAS u64*)(ws + O_FLG + (ph * 256 + idx) * 128);
    for (int s = 0; s < 400000; ++s) {
        const u64 v = __hip_atomic_load(f, __ATOMIC_RELAXED, __HIP_MEMORY_SCOPE_AGENT);
        if (__all(v == ep)) break;
        __builtin_amdgcn_s_sleep(1);
    }
    asm volatile("" ::: "memory");
}

DI u64 peek_flag(char* ws, int ph, int idx) {
    return __hip_atomic_load((GAS u64*)(ws + O_FLG + (ph * 256 + idx) * 128), __ATOMIC_RELAXED, __HIP_MEMORY_SCOPE_AGENT);
}
DI void finish_wait(char* ws, int ph, int idx, u64 ep, u64 first) {
    if (!__all(first == ep)) wait_flags(ws, ph, idx, ep);
    asm volatile("" ::: "memory");
}

constexpr float AMP = 0.05590169943749474f;
constexpr float AMP0 = 0.03952847075210474f;

struct Map { int tt, m, rb, ct, isC, g, aj; };
__host__ __device__ inline Map get_map(int id) {
    Map r;
    const int xcd = id & 7, slot = id >> 3;
    r.tt = xcd + 8 * (slot >> 4); r.m = slot & 15; r.rb = 2 * r.tt + (r.m >> 3); r.ct = r.m & 7;
    const bool aux = slot >= 24 || (slot >= 16 && ((slot >> 2) & 1) == (xcd & 1));
    r.isC = !aux;
    r.g = 20 * xcd + (slot < 16 ? slot : 16 + (slot & 3));
    r.aj = slot >= 24 ? xcd : 8 + (xcd >> 1);
    return r;
}

DI void gen_q(u32x4 (&Q)[5][5], int ct, int w, int lane) {
    const int n = lane & 15, q = lane >> 4;
#pragma unroll
    for (int mt = 0; mt < 5; ++mt) {
        const int c = 80 * ct + 16 * mt + n;
        const float pf = c == 1 ? 320.f : (float)(c >> 1);
        const float ph = ((c & 1) && c != 1) ? 0.25f : 0.f;
        const float dl = pf * (1.f / 640.f);
        const float t = pf * (float)(160 * w + 8 * q);
        const float k = floorf(t * (1.f / 640.f));
        float r = __builtin_fmaf(-k, 640.f, t) * (1.f / 640.f);
        const float d32 = fract(pf * 0.05f);
#pragma unroll
        for (int kk = 0; kk < 5; ++kk) {
            const float rb = r + ph;
            float v[8];
#pragma unroll
            for (int j = 0; j < 8; ++j) v[j] = vcos(__builtin_fmaf((float)j, dl, rb));
            Q[mt][kk] = (u32x4){pk(v[0], v[1]), pk(v[2], v[3]), pk(v[4], v[5]), pk(v[6], v[7])};
            r = fract(r + d32);
        }
    }
}

template <int NA, int WS, class MM>
DI void ktail(f32x4 (&acc)[4][NA], f32x4* sb, int w, int lane, MM mm) {
    f32x4* wr = sb + w * WS + lane;
    f32x4* rd = sb + (w ^ 2) * WS + lane;
    const f32x4* rd2 = sb + (w ^ 3) * WS + NA * 64 + lane;
    mm(2, 0); mm(2, 1); mm(3, 0); mm(3, 1);
    SB();
    mm(1, 0);
#pragma unroll
    for (int m = 0; m < NA; ++m) wr[m * 64] = acc[2][m];
    SB();
    mm(1, 1);
#pragma unroll
    for (int m = 0; m < NA; ++m) wr[(NA + m) * 64] = acc[3][m];
    SB();
    bar();
    f32x4 in1[NA], in0[NA];
#pragma unroll
    for (int m = 0; m < NA; ++m) in1[m] = rd[(NA + m) * 64];
    SB();
    mm(0, 0);
    SB();
#pragma unroll
    for (int m = 0; m < NA; ++m) rd[(NA + m) * 64] = acc[1][m] + in1[m];
    SB();
#pragma unroll
    for (int m = 0; m < NA; ++m) in0[m] = rd[m * 64];
    mm(0, 1);
    SB();
    bar();
#pragma unroll
    for (int m = 0; m < NA; ++m) in1[m] = rd2[m * 64];
#pragma unroll
    for (int m = 0; m < NA; ++m) acc[0][m] += in0[m];
    SB();
#pragma unroll
    for (int m = 0; m < NA; ++m) acc[0][m] += in1[m];
}

DI void store_xh(const f32x4 (&v)[5], rsrc_t rws, unsigned o_xh, unsigned o_st, int rb, int ct, int t, int lane, char* sb) {
    const int n = lane & 15, q = lane >> 4;
    u32x2 d[5];
    float ss = 0.f;
#pragma unroll
    for (int mt = 0; mt < 5; ++mt) {
        d[mt] = (u32x2){pk(v[mt][0], v[mt][1]), pk(v[mt][2], v[mt][3])};
        const unsigned m0 = (mt == 0 && ct == 0 && q == 0) ? (d[mt][0] & 0xffff0000u) : d[mt][0];
        ss = dot2(m0, m0, ss); ss = dot2(d[mt][1], d[mt][1], ss);
    }
    u32x2* mine = (u32x2*)(sb + (t ^ 2) * 10240) + lane;
    const u32x2* other = (const u32x2*)(sb + (t ^ 3) * 10240) + lane;
    const unsigned base = o_xh + (20 * ct + q) * GS + (2 * rb + (t >> 1)) * 256u + n * 16u;
    if (t & 1) {
        mine[0] = d[0]; mine[64] = d[1]; mine[128] = d[2];
        ss = sumq(ss);
        mine[192] = (u32x2){__float_as_uint(ss), 0u};
        bar();
        const u32x2 a = other[0], b = other[64];
        cst16(rws, base + 12 * GS, (u32x4){a[0], a[1], d[3][0], d[3][1]});
        cst16(rws, base + 16 * GS, (u32x4){b[0], b[1], d[4][0], d[4][1]});
    } else {
        mine[0] = d[3]; mine[64] = d[4];
        ss = sumq(ss);
        bar();
        const u32x2 a = other[0], b = other[64], c = other[128], s1 = other[192];
        cst16(rws, base, (u32x4){d[0][0], d[0][1], a[0], a[1]});
        cst16(rws, base + 4 * GS, (u32x4){d[1][0], d[1][1], b[0], b[1]});
        cst16(rws, base + 8 * GS, (u32x4){d[2][0], d[2][1], c[0], c[1]});
        const unsigned ln = ((t >> 1) + 2 * ((rb >> 2) & 1)) * 16 + n;
        const unsigned so = o_st + ((ct * 4 + (rb >> 3)) * 64 + ln) * 16u + (rb & 3) * 4u;
        cst4(rws, q == 0 ? so : 0xfffffff0u, pk(ss, __uint_as_float(s1[0])));
    }
}

DI int w1f_idx(int m, int lane) { return (lane & 7) * 12 + ((lane & 8) ? (96 * m + 95) >> 7 : (96 * m) >> 7); }
DI void w1f_wait(char* ws, int m, int lane, u64 ep) { wait_flags(ws, PH_A1, w1f_idx(m, lane), ep); }
DI int a3_idx(int lane) { return lane; }
DI int a3_idx2(int lane) { return 64 + (lane & 31); }
DI u32x4 w1f_load(rsrc_t rws, int m, int w, int lane, int i) { return cld16(rws, O_W1F + m * 122880u + w * 30720u + i * 1024u + lane * 16u); }
DI void w1f_put(const u32x4 (&t)[30], char* smem, int w, int lane) {
    u32x4* dst = (u32x4*)(smem + w * 30720) + lane;
#pragma unroll
    for (int i = 0; i < 30; ++i) dst[i * 64] = t[i];
}

DI void aux_dma(const Params& p, int aj, int w, int lane, char* smem, int k0, int k1) {
    const rsrc_t rw1 = mkrs(p.w1, D * F * 2);
    const unsigned lb = (unsigned)(size_t)(LAS char*)smem;
#pragma unroll
    for (int kk = k0; kk < k1; ++kk)
#pragma unroll
        for (int i = 0; i < 8; ++i) {
            const unsigned vo = (160 * w + (lane >> 4)) * (F * 2u) + aj * 256u + (((lane & 15) ^ (((lane >> 4) << 2) | (i & 3))) << 4);
            const unsigned dst = kk < 3 ? w * 24576u + kk * 8192u : 98304u + w * 16384u + (kk - 3) * 8192u;
            dma16(rw1, vo, (32 * kk + 4 * i) * (F * 2u), lb + dst + i * 1024u);
        }
}
DI void phase_aux(const Params& p, const u32x4 (&Q)[5][5], int ct, int aj, int m, u64 ep, char* smem) {
    IDS;
    const rsrc_t rw2 = mkrs(p.w2, D * F * 2), rws = mkrs(p.ws, WS_USED);
    aux_dma(p, aj, w, lane, smem, 3, 5);
    SB();
    u32x4 a2[4][2][5];
    const unsigned a2o = (128 * aj + n) * 1280u + (160 * w + 8 * q) * 2u;
#define A2LOAD(j) do { _Pragma("unroll") for (int e = 0; e < 2; ++e) { _Pragma("unroll") for (int kk = 0; kk < 5; ++kk) \
        a2[j][e][kk] = ld16(rw2, a2o + (32 * ((j) ^ w) + 16 * e) * 1280u + kk * 64u); } } while (0)
    f32x4 acc[4][10];
    const f32x4 z = {0.f, 0.f, 0.f, 0.f};
    {
        unsigned ta[4][2][2];
        {
            const int qq = (lane >> 2) & 3, pp = lane & 3;
#pragma unroll
            for (int j = 0; j < 4; ++j)
#pragma unroll
                for (int e = 0; e < 2; ++e)
#pragma unroll
                    for (int hh = 0; hh < 2; ++hh) {
                        const int row = 8 * q + 4 * hh + qq, ch = 4 * (j ^ w) + 2 * e + (pp >> 1);
                        ta[j][e][hh] = w * 24576u + 256u * row + 16u * (ch ^ ((qq << 2) | ((2 * q + hh) & 3))) + 8u * (pp & 1);
                    }
        }
        auto rdb = [&](int kk, int j, int e) -> u32x4 {
            const unsigned ko = kk < 3 ? kk * 8192u : (kk - 3) * 8192u;
            const u32x2 a = trd(smem + ta[j][e][0] + ko), b = trd(smem + ta[j][e][1] + ko);
            return (u32x4){a[0], a[1], b[0], b[1]};
        };
        u32x4 bc = rdb(0, 0, 0);
#pragma unroll
        for (int s = 0; s < 24; ++s) {
            const int kk = s >> 3, j = (s >> 1) & 3, e = s & 1;
            u32x4 bn;
            if (s < 23) bn = rdb((s + 1) >> 3, ((s + 1) >> 1) & 3, (s + 1) & 1);
#pragma unroll
            for (int mt = 0; mt < 5; ++mt) acc[j][5 * e + mt] = mfma(Q[mt][kk], bc, kk == 0 ? z : acc[j][5 * e + mt]);
            if (s < 23) bc = bn;
            SB();
        }

        asm volatile("s_waitcnt vmcnt(0)" ::: "memory");
#pragma unroll
        for (int j = 0; j < 4; ++j)
#pragma unroll
            for (int e = 0; e < 2; ++e)
#pragma unroll
                for (int hh = 0; hh < 2; ++hh) ta[j][e][hh] += 98304u - w * 8192u;
        u32x4 bq[2][2];
        auto nextj = [](int j) { return j == 2 ? 3 : j == 3 ? 1 : 0; };
        bq[0][0] = rdb(3, 2, 0); bq[0][1] = rdb(3, 2, 1);
        ktail<10, 1536>(acc, (f32x4*)smem, w, lane, [&](int j, int part) {
            u32x4 nb[2];
            const bool more = part == 0 || j != 0;
            if (more) { nb[0] = rdb(part ? 3 : 4, part ? nextj(j) : j, 0); nb[1] = rdb(part ? 3 : 4, part ? nextj(j) : j, 1); }
#pragma unroll
            for (int e = 0; e < 2; ++e)
#pragma unroll
                for (int mt = 0; mt < 5; ++mt) acc[j][5 * e + mt] = mfma(Q[mt][3 + part], bq[0][e], acc[j][5 * e + mt]);
            if (more) { bq[0][0] = nb[0]; bq[0][1] = nb[1]; }
            if (j == 1 && part == 1) { A2LOAD(2); }
            if (j == 0 && part == 1) { A2LOAD(3); }
        });

        A2LOAD(1);
#pragma unroll
        for (int e = 0; e < 2; ++e) {
            u32x2 d[5];
#pragma unroll
            for (int mt = 0; mt < 5; ++mt) {
                f32x4 v = acc[0][5 * e + mt] * AMP;
                if (mt == 0 && ct == 0 && q == 0) { v[0] = 0.f; v[1] *= AMP0 / AMP; }
                d[mt] = (u32x2){pk(v[0], v[1]), pk(v[2], v[3])};
            }
            const unsigned o = O_W1F + (((8 * aj + 2 * w + e) * 20 + ((5 * ct) >> 1)) * 64 + lane) * 16u;
            if (ct & 1) {
                cst8(rws, o + 8, d[0]);
                cst16(rws, o + 1024, (u32x4){d[1][0], d[1][1], d[2][0], d[2][1]});
                cst16(rws, o + 2048, (u32x4){d[3][0], d[3][1], d[4][0], d[4][1]});
            } else {
                cst16(rws, o, (u32x4){d[0][0], d[0][1], d[1][0], d[1][1]});
                cst16(rws, o + 1024, (u32x4){d[2][0], d[2][1], d[3][0], d[3][1]});
                cst8(rws, o + 2048, d[4]);
            }
        }
    }
    SB();

    u32x4 t[30];
    {
        const rsrc_t rb2 = mkrs(p.b2, aj == 0 ? D * 2 : 0);
        u32x4 b2f[5];
        ktail<10, 1536>(acc, (f32x4*)smem, w, lane, [&](int j, int part) {
#pragma unroll
            for (int kk = part ? 3 : 0; kk < (part ? 5 : 3); ++kk)
#pragma unroll
                for (int e = 0; e < 2; ++e)
#pragma unroll
                    for (int mt = 0; mt < 5; ++mt) acc[j][5 * e + mt] = mfma(a2[j][e][kk], Q[mt][kk], kk == 0 ? z : acc[j][5 * e + mt]);
            if (j == 2 && part == 1) {
                SB();
                publish(p.ws, PH_A1, ct * 12 + aj, ep);

                A2LOAD(0);
#pragma unroll
                for (int kk = 0; kk < 5; ++kk) b2f[kk] = ld16(rb2, (160 * w + 32 * kk + 8 * q) * 2u);
            }
            if (j == 1 && part == 1) { SB(); w1f_wait(p.ws, m, lane, ep); }
            if (j == 0) {
#pragma unroll
                for (int i = 10 * part; i < 10 + 10 * part; ++i) t[i] = w1f_load(rws, m, w, lane, i);
            }
        });

#pragma unroll
        for (int i = 20; i < 30; ++i) t[i] = w1f_load(rws, m, w, lane, i);
#pragma unroll
        for (int mt = 0; mt < 5; ++mt) {
            const float am = (mt == 0 && ct == 0 && n < 2) ? AMP0 : AMP;
            const f32x4 v0 = acc[0][mt] * am, v1 = acc[0][5 + mt] * am;
            cst16(rws, O_W2F + (((5 * ct + mt) * 48 + 4 * aj + w) * 64 + lane) * 16u, (u32x4){pk(v0[0], v0[1]), pk(v0[2], v0[3]), pk(v1[0], v1[1]), pk(v1[2], v1[3])});
        }
        if (aj == 0) {
            f32x4* pb = (f32x4*)(smem + 20480);
#pragma unroll
            for (int mt = 0; mt < 5; ++mt) {
                f32x4 a = z;
#pragma unroll
                for (int kk = 0; kk < 5; ++kk) a = mfma(Q[mt][kk], b2f[kk], a);
                if (n == 0) pb[w * 20 + mt * 4 + q] = a;
            }
            bar();
            if (tid < 20) {
                f32x4 s = (pb[lane] + pb[20 + lane] + pb[40 + lane] + pb[60 + lane]) * AMP;
                if (lane == 0 && ct == 0) { s[0] *= AMP0 / AMP; s[1] *= AMP0 / AMP; }
                cst16(rws, O_B2P + (80 * ct + 4 * lane) * 4u, __builtin_bit_cast(u32x4, s));
            }
        }
    }
    publish(p.ws, PH_A2, ct * 12 + aj, ep);
    w1f_put(t, smem, w, lane);
}

DI void phase_a3(const Params& p, int ar, u64 ep, char* scr) {
    IDS;
    const rsrc_t rw2 = mkrs(p.w2, D * F * 2), rws = mkrs(p.ws, WS_USED);
    const int u = ar >> 1, half = ar & 1;
    const unsigned lb = (unsigned)(size_t)(LAS char*)scr;
#pragma unroll
    for (int i = 0; i < 5; ++i) {
        const unsigned o = (w * 5 + i) * 1024u + lane * 16u, row = o / 640u;
        dma16(rw2, (32 * u + row) * 1280u + half * 640u + (o - row * 640u), 0u, lb + (w * 5 + i) * 1024u);
    }
#pragma unroll
    for (int i = 0; i < 3; ++i) {
        const int fi = ar * 4 + w + 384 * i;
        if (fi < 800) {
            const int T = fi / 20, kk = fi - 20 * T;
            const float df = (float)(16 * T + n);
            u32x4 v;
#pragma unroll
            for (int hh = 0; hh < 2; ++hh)
#pragma unroll
                for (int pr = 0; pr < 2; ++pr) {
                    const int c = 32 * kk + 16 * hh + 4 * q + 2 * pr;
                    const float t = (float)(c >> 1) * df;
                    const float k = floorf(t * (1.f / 640.f));
                    const float r = __builtin_fmaf(-k, 640.f, t) * (1.f / 640.f);
                    float v0 = vcos(r) * AMP, v1 = vcos(r + 0.25f) * AMP;
                    if (c == 0) { v0 = AMP0; v1 = (n & 1) ? -AMP0 : AMP0; }
                    v[2 * hh + pr] = pk(v0, v1);
                }
            cst16(rws, O_QT + fi * 1024u + lane * 16u, v);
        }
    }
    asm volatile("s_waitcnt vmcnt(0)" ::: "memory");
    bar();
#pragma unroll
    for (int i = 0; i < 5; ++i) {
        const int tl = 5 * w + i;
        const char* a = scr + (4 * q + ((lane >> 2) & 3)) * 640 + tl * 32 + (lane & 3) * 8;
        const u32x2 lo = trd(a), hi = trd(a + 16 * 640);
        cst16(rws, O_W2T + (((20 * half + tl) * 48 + u) * 64 + lane) * 16u, (u32x4){lo[0], lo[1], hi[0], hi[1]});
    }
    publish(p.ws, PH_A3, ar, ep);
}

template <int L>
DI void phase_c(const Params& p, int g, int m1, u64 ep, char* lds0, char* smem) {
    constexpr unsigned o_in = L ? O_XH2 : O_XH0, o_st = L ? O_ST2 : O_ST0, o_out = L ? O_XH3 : O_XH1;
    constexpr int SO = L ? 28 : 8;
    IDS;
    const rsrc_t rws = mkrs(p.ws, WS_USED);
    const int c = w >> 1, kh = w & 1;
    u32x4 B1r[2], B1i[2], A3[4][4], A6[2][2];
    f32x2 twc[4][2], tws[4][2];
    u64 fv;
    {
        const float ka = (float)(16 * kh + n);
#pragma unroll
        for (int ks = 0; ks < 2; ++ks)
#pragma unroll
            for (int m = 0; m < 4; ++m) {
                const float r = (float)(16 * ks + 4 * q + m) * (1.f / 32.f) * ka;
                const float cs = vcos(r), sn = vsin(r);
                B1r[ks][m] = pk(cs, sn); B1i[ks][m] = pk(-sn, cs);
            }
#pragma unroll
        for (int bt = 0; bt < 4; ++bt)
#pragma unroll
            for (int r = 0; r < 4; ++r) {
                const float a = (float)(16 * bt + 4 * q + r) * ka * (1.f / 2048.f);
                twc[bt][r >> 1][r & 1] = vcos(a); tws[bt][r >> 1][r & 1] = vsin(a);
            }
        SB();
        fv = peek_flag(p.ws, L ? PH_G2 : PH_P0, tid);
        SB();
#pragma unroll
        for (int bt = 0; bt < 4; ++bt)
#pragma unroll
            for (int m = 0; m < 4; ++m) {
                const float a = (float)n * (float)(16 * bt + 4 * q + m) * (1.f / 64.f);
                const float cs = vcos(a), sn = vsin(a);
#pragma unroll
                for (int mt = 0; mt < 4; ++mt) {
                    const int k4 = (mt * m) & 3;
                    A3[mt][bt][m] = k4 == 0 ? pk(cs, sn) : k4 == 1 ? pk(-sn, cs) : k4 == 2 ? pk(-cs, -sn) : pk(sn, -cs);
                }
            }
        const float rio = (float)(n & 1) * -0.25f;
#pragma unroll
        for (int rt = 0; rt < 2; ++rt)
#pragma unroll
            for (int ks = 0; ks < 2; ++ks)
#pragma unroll
                for (int m = 0; m < 4; ++m) {
                    const float a = (float)(16 * (w >> 1) + 8 * rt + (n >> 1)) * (float)(16 * ks + 4 * q + m) * (1.f / 32.f) + rio;
                    A6[rt][ks][m] = pk(vcos(a) * (1.f / 2048.f), vcos(a + 0.25f) * (1.f / 2048.f));
                }
#pragma unroll
        for (int i = 0; i < 4; ++i) {
            if (i < 2) { PIN(B1r[i]); PIN(B1i[i]); PIN(A6[i][0]); PIN(A6[i][1]); }
            PIN(A3[i][0]); PIN(A3[i][1]); PIN(A3[i][2]); PIN(A3[i][3]);
            PIN(twc[i][0]); PIN(twc[i][1]); PIN(tws[i][0]); PIN(tws[i][1]);
        }
    }
    SB();

    finish_wait(p.ws, L ? PH_G2 : PH_P0, tid, ep, fv);
    bar();

    u32x4 xin[4], st[8], wf[4];
    {
        const int h = q & 1, hi = q >> 1;
#pragma unroll
        for (int cp = 0; cp < 8; ++cp) st[cp] = cld16(rws, o_st + ((cp * 4 + w) * 64 + lane) * 16u);
#pragma unroll
        for (int ii = 0; ii < 4; ++ii) xin[ii] = cld16(rws, o_in + g * GS + (2 * (8 * w + 4 * hi + ii) + h) * 256u + n * 16u);
#pragma unroll
        for (int mt = 0; mt < 4; ++mt) wf[mt] = cld16(rws, O_WFQ + ((2 * g + c) * 512 + (kh * 4 + mt) * 64 + lane) * 16u);
    }
    SB();
    u32x4* FR = (u32x4*)smem;
    u32x4* RAW = (u32x4*)(smem + 16384);
    {
        const int h = q & 1, hi = q >> 1;
        float rs[4][2];
        const unsigned sel0 = (unsigned)fresh_s(0x00003f80), sel1 = (unsigned)fresh_s(0x3f800000);
#pragma unroll
        for (int ii = 0; ii < 4; ++ii)
#pragma unroll
            for (int hf = 0; hf < 2; ++hf) {
                float s = 0.f;
#pragma unroll
                for (int cp = 0; cp < 8; ++cp) s = dot2(st[cp][ii], hf ? sel1 : sel0, s);
                rs[ii][hf] = __builtin_amdgcn_rsqf(__builtin_fmaf(s, 1.f / 640.f, 1e-5f));
            }
#pragma unroll
        for (int ii = 0; ii < 4; ++ii) RAW[((8 * w + 4 * hi + ii) * 2 + h) * 16 + n] = xin[ii];
        const int aq = 2 * w + hi;
#pragma unroll
        for (int hf = 0; hf < 2; ++hf)
#pragma unroll
            for (int cc = 0; cc < 2; ++cc) {
                u32x4 o;
#pragma unroll
                for (int ii = 0; ii < 4; ++ii) {
                    const unsigned u = xin[ii][hf * 2 + cc];
                    float re = blo(u) * rs[ii][hf];
                    if (cc == 0 && g == 0) re = 0.f;
                    o[ii] = pk(re, bhi(u) * rs[ii][hf]);
                }
                FR[((cc * 4 + 2 * h + hf) * 2 + (aq >> 2)) * 64 + (aq & 3) * 16 + n] = o;
            }
    }
    bar();

    f32x4 tr[4], ti[4];
    {
        u32x4 a[4][2];
#pragma unroll
        for (int bt = 0; bt < 4; ++bt) { a[bt][0] = FR[((c * 4 + bt) * 2 + 0) * 64 + lane]; a[bt][1] = FR[((c * 4 + bt) * 2 + 1) * 64 + lane]; }
        SB();
#pragma unroll
        for (int bt = 0; bt < 4; ++bt) {
            const f32x4 z = {0.f, 0.f, 0.f, 0.f};
            tr[bt] = mfma(a[bt][1], B1r[1], mfma(a[bt][0], B1r[0], z));
            ti[bt] = mfma(a[bt][1], B1i[1], mfma(a[bt][0], B1i[0], z));
        }
    }
    bar();
    u32x4 P[4], Pn[4];
#pragma unroll
    for (int bt = 0; bt < 4; ++bt)
#pragma unroll
        for (int r2 = 0; r2 < 2; ++r2) {
            const f32x2 a = {tr[bt][2 * r2], tr[bt][2 * r2 + 1]}, b = {ti[bt][2 * r2], ti[bt][2 * r2 + 1]};
            const f32x2 xr = a * twc[bt][r2] + b * tws[bt][r2], xi = b * twc[bt][r2] - a * tws[bt][r2];
            P[bt][2 * r2] = pk(xr[0], xi[0]); P[bt][2 * r2 + 1] = pk(xr[1], xi[1]);
            Pn[bt][2 * r2] = pk(xi[0], -xr[0]); Pn[bt][2 * r2 + 1] = pk(xi[1], -xr[1]);
        }
    u32x4 R[4], Rn[4];
    u64 fv2 = ep;
#pragma unroll
    for (int mt = 0; mt < 4; ++mt) {
        if (mt == 2) { SB(); fv = peek_flag(p.ws, L ? PH_A3 : PH_A1, L ? a3_idx(lane) : w1f_idx(m1, lane)); if (L) fv2 = peek_flag(p.ws, PH_A3, a3_idx2(lane)); SB(); }
        f32x4 zr = {0.f, 0.f, 0.f, 0.f}, zi = {0.f, 0.f, 0.f, 0.f};
#pragma unroll
        for (int bt = 0; bt < 4; ++bt) { zr = mfma(A3[mt][bt], P[bt], zr); zi = mfma(A3[mt][bt], Pn[bt], zi); }
        unsigned pr[4], pi[4];
#pragma unroll
        for (int r = 0; r < 4; ++r) {
            const float fr = blo(wf[mt][r]), fi = bhi(wf[mt][r]);
            const float yr = zr[r] * fr - zi[r] * fi, yi = zr[r] * fi + zi[r] * fr;
            pr[r] = pk(yr, -yi); pi[r] = pk(yi, yr);
        }
        R[mt] = (u32x4){pr[0], pr[1], pr[2], pr[3]}; Rn[mt] = (u32x4){pi[0], pi[1], pi[2], pi[3]};
    }

    u32x4 t1[L ? 1 : 30];
    SB();
    finish_wait(p.ws, L ? PH_A3 : PH_A1, L ? a3_idx(lane) : w1f_idx(m1, lane), ep, fv);
    if (L) finish_wait(p.ws, PH_A3, a3_idx2(lane), ep, fv2);
    SB();
    unsigned* TR = (unsigned*)smem;
#pragma unroll
    for (int bt = 0; bt < 4; ++bt) {
        f32x4 ur = {0.f, 0.f, 0.f, 0.f}, ui = {0.f, 0.f, 0.f, 0.f};
#pragma unroll
        for (int kt = 0; kt < 4; ++kt) { ur = mfma(A3[bt][kt], R[kt], ur); ui = mfma(A3[bt][kt], Rn[kt], ui); }
        if constexpr (!L) {
#pragma unroll
            for (int i = 5 * bt; i < 5 * bt + 5; ++i) t1[i] = w1f_load(rws, m1, w, lane, i);
        }
#pragma unroll
        for (int r2 = 0; r2 < 2; ++r2) {
            const f32x2 a = {ur[2 * r2], ur[2 * r2 + 1]}, b = {ui[2 * r2], ui[2 * r2 + 1]};
            const f32x2 xr = a * twc[bt][r2] - b * tws[bt][r2], xi = b * twc[bt][r2] + a * tws[bt][r2];
#pragma unroll
            for (int e = 0; e < 2; ++e) {
                const int r = 2 * r2 + e;
                const int b_ = 16 * bt + 4 * q + r;
                const int kq = 4 * kh + (n >> 2), f = (2 * q + r2) & 7;
                TR[c * 2048 + b_ * 32 + ((kq ^ f) << 2) + (n & 3)] = pk(xr[e], xi[e]);
            }
        }
        SB();
    }
    if constexpr (!L) {
#pragma unroll
        for (int i = 20; i < 30; ++i) t1[i] = w1f_load(rws, m1, w, lane, i);
    }
    bar();

    {
        const int h = w & 1, ah = w >> 1;
        const u32x4* TRv = (const u32x4*)smem;
        u32x4 bq[2][2][2], raw[2][2];
#pragma unroll
        for (int hf = 0; hf < 2; ++hf)
#pragma unroll
            for (int cc = 0; cc < 2; ++cc) {
                const int b_ = 16 * (2 * h + hf) + n, f = (n >> 1) & 7;
                bq[hf][cc][0] = TRv[cc * 512 + b_ * 8 + ((0 + q) ^ f)]; bq[hf][cc][1] = TRv[cc * 512 + b_ * 8 + ((4 + q) ^ f)];
            }
#pragma unroll
        for (int rt = 0; rt < 2; ++rt)
#pragma unroll
            for (int e = 0; e < 2; ++e) raw[rt][e] = RAW[((16 * ah + 8 * rt + 2 * q + e) * 2 + h) * 16 + n];
        SB();
        u32x4 o[2][2];
#pragma unroll
        for (int rt = 0; rt < 2; ++rt) {
#pragma unroll
            for (int hf = 0; hf < 2; ++hf)
#pragma unroll
                for (int cc = 0; cc < 2; ++cc) {
                    const unsigned r0 = raw[rt][0][hf * 2 + cc], r1 = raw[rt][1][hf * 2 + cc];
                    f32x4 y = {0.f, 0.f, 0.f, 0.f};
                    y = mfma(A6[rt][0], bq[hf][cc][0], y); y = mfma(A6[rt][1], bq[hf][cc][1], y);
                    if (cc == 0 && g == 0) { y[0] = 0.f; y[2] = 0.f; }
                    o[rt][0][hf * 2 + cc] = pk(y[0] + blo(r0), y[1] + bhi(r0));
                    o[rt][1][hf * 2 + cc] = pk(y[2] + blo(r1), y[3] + bhi(r1));
                }
#pragma unroll
            for (int e = 0; e < 2; ++e)
                cst16(rws, o_out + g * GS + (2 * (16 * ah + 8 * rt + 2 * q + e) + h) * 256u + n * 16u, o[rt][e]);
        }
    }

    publish(p.ws, L ? PH_C2 : PH_C1, g, ep);

    if constexpr (!L) w1f_put(t1, lds0, w, lane);
}

template <int L>
DI void phase_g1(const Params& p, int tt, int fs, int rb, u32x4 (&W)[5][12], unsigned wo, u32x4 (&xg)[2][5][2], u64 ep, char* smem) {
    constexpr unsigned o_in = L ? O_XH3 : O_XH1, o_hf = L ? O_HF2 : O_HF1;
    constexpr int SO = L ? 36 : 17;
    constexpr int DP = 8;
    IDS;
    const rsrc_t rws = mkrs(p.ws, WS_USED), rb1 = mkrs(p.b1, F * 2);
    u32x2 bb[6];
#pragma unroll
    for (int mt = 0; mt < 6; ++mt) bb[mt] = ld8(rb1, (96 * fs + 16 * mt + 4 * q) * 2u);
    {
        const int e = w * 40 + (lane < 40 ? lane : 0);
        wait_flags(p.ws, L ? PH_C2 : PH_C1, e, ep);
    }
    bar();

    const int blk = 4 * tt + w;
    const unsigned xo = o_in + q * GS + blk * 256u + n * 16u;
    const unsigned go = O_XH3 + (40 * w + q) * GS + (2 * rb) * 256u + n * 16u;
    u32x4 l[DP][2];
#pragma unroll
    for (int kk = 0; kk < DP; ++kk)
#pragma unroll
        for (int e = 0; e < 2; ++e) l[kk][e] = cld16(rws, xo + (8 * kk + 4 * e) * GS);
    SB();
    const u32x4* WL = (const u32x4*)smem + lane;
    f32x4 acc[6][2], gr0 = {0.f, 0.f, 0.f, 0.f}, gr1 = {0.f, 0.f, 0.f, 0.f};
#pragma unroll
    for (int mt = 0; mt < 6; ++mt) { acc[mt][0] = (f32x4){0.f, 0.f, 0.f, 0.f}; acc[mt][1] = (f32x4){0.f, 0.f, 0.f, 0.f}; }
    u32x4 A[2][6];
#pragma unroll
    for (int mt = 0; mt < 6; ++mt) A[0][mt] = WL[(mt * 20) * 64];
    u32x4 f0 = {l[0][0][0], l[0][0][1], l[0][1][0], l[0][1][1]}, f1 = {l[0][0][2], l[0][0][3], l[0][1][2], l[0][1][3]};
    if (q == 0) { f0[0] &= 0xffff0000u; f1[0] &= 0xffff0000u; }
    SB();
#pragma unroll
    for (int kk = 0; kk < 20; ++kk) {
        u32x4 g0, g1;
#pragma unroll
        for (int mt = 0; mt < 6; ++mt) {
            acc[mt][0] = mfma(A[kk & 1][mt], f0, acc[mt][0]);
            acc[mt][1] = mfma(A[kk & 1][mt], f1, acc[mt][1]);
            if (kk < 19) A[(kk + 1) & 1][mt] = WL[(mt * 20 + kk + 1) * 64];
            if (mt == 0) gr0 = mfma(f0, f0, gr0);
            if (mt == 1) gr1 = mfma(f1, f1, gr1);
            if (mt == 2 && kk < 19) {
                const int s = (kk + 1) % DP;
                g0 = (u32x4){l[s][0][0], l[s][0][1], l[s][1][0], l[s][1][1]};
                g1 = (u32x4){l[s][0][2], l[s][0][3], l[s][1][2], l[s][1][3]};
            }
            if ((mt == 0 || mt == 3) && kk + DP < 20) l[kk % DP][mt ? 1 : 0] = cld16(rws, xo + (8 * (kk + DP) + 4 * (mt ? 1 : 0)) * GS);
            if (L && mt == 5) xg[kk / 10][(kk % 10) >> 1][kk & 1] = cld16(rws, go + (8 * ((kk % 10) >> 1) + 4 * (kk & 1)) * GS + (((kk / 10) ^ (w >> 1)) * 256u));
            SB();
        }
        if (kk < 19) { f0 = g0; f1 = g1; }
        }

    float rs[2];
    {
        const int r = n & 3;
        const float s0 = r == 0 ? gr0[0] : r == 1 ? gr0[1] : r == 2 ? gr0[2] : gr0[3];
        const float s1 = r == 0 ? gr1[0] : r == 1 ? gr1[1] : r == 2 ? gr1[2] : gr1[3];
        const bool dg = q == (n >> 2);
        rs[0] = __builtin_amdgcn_rsqf(__builtin_fmaf(sumq(dg ? s0 : 0.f), 1.f / 640.f, 1e-5f));
        rs[1] = __builtin_amdgcn_rsqf(__builtin_fmaf(sumq(dg ? s1 : 0.f), 1.f / 640.f, 1e-5f));
    }
    constexpr float C0 = -2.3022081985f, C1 = -0.1029432397f;
#pragma unroll
    for (int hf = 0; hf < 2; ++hf)
#pragma unroll
        for (int u = 0; u < 3; ++u) {
            u32x4 o;
#pragma unroll
            for (int e = 0; e < 2; ++e) {
                const int mt = 2 * u + e, gi = (hf * 3 + u) * 2 + e;
#pragma unroll
                for (int i = 3 * gi; i < 3 * gi + 3 && i < 35; ++i) W[i % 5][i / 5] = cld16(rws, wo + ((i % 5) * 48 + i / 5) * 1024u);
                float hv[4];
#pragma unroll
                for (int r = 0; r < 4; ++r) {
                    const float bv = (r & 1) ? bhi(bb[mt][r >> 1]) : blo(bb[mt][r >> 1]);
                    const float x = __builtin_fmaf(acc[mt][hf][r], rs[hf], bv);
                    const float ex = __builtin_amdgcn_exp2f(x * __builtin_fmaf(x * x, C1, C0));
                    hv[r] = x * __builtin_amdgcn_rcpf(1.f + ex);
                }
                o[2 * e] = pk(hv[0], hv[1]); o[2 * e + 1] = pk(hv[2], hv[3]);
                SB();
            }
            cst16(rws, o_hf + (((2 * blk + hf) * 48 + 3 * fs + u) * 64 + lane) * 16u, o);
        }

    publish(p.ws, L ? PH_G1B : PH_G1A, tt * 16 + fs, ep);

}

#define HSTREAM(o_hf, ph, stamp) \
    wait_flags(p.ws, ph, (rb >> 1) * 16 + 4 * w + (lane & 3), ep); \
 \
    u32x4 hb[3][4]; \
    const unsigned ho = o_hf + ((4 * rb * 48 + 12 * w) * 64 + lane) * 16u; \
    _Pragma("unroll") for (int u = 0; u < 3; ++u) { _Pragma("unroll") for (int j = 0; j < 4; ++j) hb[u][j] = cld16(rws, ho + ((j ^ w) * 48 + u) * 1024u); } \
    SB()

DI void phase_g2(const Params& p, int rb, int ct, u32x4 (&W)[5][12], unsigned wo, u64 ep, char* scr) {
    IDS;
    const rsrc_t rws = mkrs(p.ws, WS_USED);
#pragma unroll
    for (int i = 35; i < 60; ++i) W[i % 5][i / 5] = cld16(rws, wo + ((i % 5) * 48 + i / 5) * 1024u);
    SB();
    HSTREAM(O_HF1, PH_G1A, 21);
    f32x4 acc[4][5];
    const f32x4 z = {0.f, 0.f, 0.f, 0.f};
    u32x2 res[5]; f32x4 bv[5];
#pragma unroll
    for (int u = 0; u < 10; ++u) {
#pragma unroll
        for (int j = 0; j < 4; ++j) {
#pragma unroll
            for (int mt = 0; mt < 5; ++mt) acc[j][mt] = mfma(W[mt][u], hb[u % 3][j], u == 0 ? z : acc[j][mt]);
            if (u + 3 < 12) hb[u % 3][j] = cld16(rws, ho + ((j ^ w) * 48 + u + 3) * 1024u);
            SB();
        }
        if (u == 8) {
#pragma unroll
            for (int mt = 0; mt < 5; ++mt) {
                res[mt] = cld8(rws, O_XH1 + (20 * ct + 4 * mt + q) * GS + (2 * rb + (w >> 1)) * 256u + n * 16u + (w & 1) * 8u);
                bv[mt] = __builtin_bit_cast(f32x4, cld16(rws, O_B2P + (80 * ct + 16 * mt + 4 * q) * 4u));
            }
            SB();
        }
        }
    ktail<5, 640>(acc, (f32x4*)scr, w, lane, [&](int j, int part) {
#pragma unroll
        for (int mt = 0; mt < 5; ++mt) acc[j][mt] = mfma(W[mt][10 + part], hb[(10 + part) % 3][j], acc[j][mt]);
    });

#pragma unroll
    for (int mt = 0; mt < 5; ++mt) {
        acc[0][mt] += bv[mt];
        acc[0][mt][0] += blo(res[mt][0]); acc[0][mt][1] += bhi(res[mt][0]); acc[0][mt][2] += blo(res[mt][1]); acc[0][mt][3] += bhi(res[mt][1]);
    }
    store_xh(acc[0], rws, O_XH2, O_ST2, rb, ct, w, lane, scr);

    publish(p.ws, PH_G2, rb * 8 + ct, ep);

}

DI void phase_g4(const Params& p, int rb, int ct, u32x4 (&W)[5][12], unsigned wo, const u32x4 (&QT)[5][5], const u32x4 (&xg)[2][5][2], u64 ep, char* scr) {
    IDS;
    const rsrc_t rws = mkrs(p.ws, WS_USED), rb2 = mkrs(p.b2, D * 2), rout = mkrs(p.out, S * D * 2);
    f32x4 acc[4][5];
    const f32x4 z = {0.f, 0.f, 0.f, 0.f};
    const bool odd = w & 1;
#pragma unroll
    for (int b = 0; b < 2; ++b)
#pragma unroll
        for (int kk = 0; kk < 5; ++kk) {
            const u32x4 x0 = xg[b][kk][0], x1 = xg[b][kk][1];
            const u32x4 lo = {x0[0], x0[1], x1[0], x1[1]}, hi = {x0[2], x0[3], x1[2], x1[3]};
            const u32x4 fa = odd ? hi : lo, fb = odd ? lo : hi;
#pragma unroll
            for (int mt = 0; mt < 5; ++mt) acc[2 * b][mt] = mfma(QT[mt][kk], fa, kk == 0 ? z : acc[2 * b][mt]);
#pragma unroll
            for (int mt = 0; mt < 5; ++mt) acc[2 * b + 1][mt] = mfma(QT[mt][kk], fb, kk == 0 ? z : acc[2 * b + 1][mt]);
            const int s = b * 5 + kk;
#pragma unroll
            for (int i = 35 + (5 * s) / 2; i < 35 + (5 * (s + 1)) / 2; ++i) W[i % 5][i / 5] = cld16(rws, wo + ((i % 5) * 48 + i / 5) * 1024u);
            SB();
        }

    HSTREAM(O_HF2, PH_G1B, 41);
    u32x2 bb[5];
#pragma unroll
    for (int u = 0; u < 10; ++u) {
#pragma unroll
        for (int j = 0; j < 4; ++j) {
#pragma unroll
            for (int mt = 0; mt < 5; ++mt) acc[j][mt] = mfma(W[mt][u], hb[u % 3][j], acc[j][mt]);
            if (u + 3 < 12) hb[u % 3][j] = cld16(rws, ho + ((j ^ w) * 48 + u + 3) * 1024u);
            SB();
        }
        if (u == 8) {
#pragma unroll
            for (int mt = 0; mt < 5; ++mt) bb[mt] = ld8(rb2, (80 * ct + 16 * mt + 4 * q) * 2u);
            SB();
        }
        }
    ktail<5, 640>(acc, (f32x4*)scr, w, lane, [&](int j, int part) {
#pragma unroll
        for (int mt = 0; mt < 5; ++mt) acc[j][mt] = mfma(W[mt][10 + part], hb[(10 + part) % 3][j], acc[j][mt]);
    });

#pragma unroll
    for (int mt = 0; mt < 5; ++mt) {
        const unsigned d0 = pk(acc[0][mt][0] + blo(bb[mt][0]), acc[0][mt][1] + bhi(bb[mt][0]));
        const unsigned d1 = pk(acc[0][mt][2] + blo(bb[mt][1]), acc[0][mt][3] + bhi(bb[mt][1]));
        st8(rout, (64 * rb + 16 * w + n) * 1280u + (80 * ct + 16 * mt + 4 * q) * 2u, (u32x2){d0, d1});
    }

}

__global__ void __launch_bounds__(256, 1) k_all(Params p) {
    __shared__ __attribute__((aligned(16))) char smem[163840];
    char* scr = smem + LDS_SCR;

    unsigned pf_code;
    {
        u64 pc; unsigned len;
        asm volatile("s_getpc_b64 %0\nkall_pf_begin:\n\ts_mov_b32 %1, kall_pf_end-kall_pf_begin" : "=s"(pc), "=s"(len));
        pf_code = ld4(mkrs((const void*)pc, len), (blockIdx.x >> 3) * 4096u + threadIdx.x * 16u);
    }
#define MAP const int id = fresh_s(blockIdx.x); const Map mp = get_map(id)
    GAS u64* epw = (GAS u64*)(p.ws + O_EPO);
    const u64 ep_in = __hip_atomic_load(epw, __ATOMIC_RELAXED, __HIP_MEMORY_SCOPE_AGENT);

    u32x4 Q[5][5];
    {
        MAP; IDS;
        const rsrc_t rws = mkrs(p.ws, WS_USED);
        const rsrc_t rx = mkrs(p.x, S * D * 2);
        u32x4 xf[5][4];
#pragma unroll
        for (int kk = 0; kk < 5; ++kk)
#pragma unroll
            for (int j = 0; j < 4; ++j)
                xf[kk][j] = ld16(rx, (64 * mp.rb + 16 * (j ^ w) + n) * 1280u + (160 * w + 32 * kk + 8 * q) * 2u);
        SB();

        const rsrc_t rwr = mkrs(p.wr, S * 321 * 2), rwi = mkrs(p.wi, S * 321 * 2);
        const int rg = mp.g / 5, cg = mp.g - 5 * rg;
        const int ks0 = 16 * (rg >> 4) + 32 * (16 * ((rg >> 2) & 3) + 4 * (rg & 3));
        unsigned fr[2][4], fi[2][4], ny[4];
        {
            const unsigned o1 = mp.isC ? ((ks0 + 4 * w + q) * 321 + 64 * cg + 2 * n) * 2u : 0x80000000u;
#pragma unroll
            for (int it = 0; it < 2; ++it)
#pragma unroll
                for (int jj = 0; jj < 4; ++jj) {
                    fr[it][jj] = __builtin_amdgcn_raw_buffer_load_b32(rwr, o1 + it * 64, jj * 32 * 321 * 2, 0);
                    fi[it][jj] = __builtin_amdgcn_raw_buffer_load_b32(rwi, o1 + it * 64, jj * 32 * 321 * 2, 0);
                }
            const bool nyq = mp.isC && cg == 0 && w == 1;
            const int ks = ks0 + (lane >> 2) + 32 * (lane & 3), kn = (2048 - ks) & 2047;
            const unsigned oa = nyq ? (ks * 321 + 320) * 2u : 0x80000000u, ob = nyq ? (kn * 321 + 320) * 2u : 0x80000000u;
            ny[0] = ld2(rwr, oa); ny[1] = ld2(rwr, ob); ny[2] = ld2(rwi, oa); ny[3] = ld2(rwi, ob);
        }
        SB();

        gen_q(Q, mp.ct, w, lane);
#pragma unroll
        for (int mt = 0; mt < 5; ++mt)
#pragma unroll
            for (int kk = 0; kk < 5; ++kk) PIN(Q[mt][kk]);
        SB();

        __builtin_amdgcn_s_waitcnt(0x4F74);
        SB();
        if (!mp.isC) aux_dma(p, mp.aj, w, lane, smem, 0, 3);
        SB();

        f32x4 acc[4][5];
        const f32x4 z = {0.f, 0.f, 0.f, 0.f};
#pragma unroll
        for (int kk = 0; kk < 3; ++kk)
#pragma unroll
            for (int j = 0; j < 4; ++j)
#pragma unroll
                for (int mt = 0; mt < 5; ++mt) acc[j][mt] = mfma(Q[mt][kk], xf[kk][j], kk == 0 ? z : acc[j][mt]);
        SB();

        ktail<5, 640>(acc, (f32x4*)scr, w, lane, [&](int j, int part) {
#pragma unroll
            for (int mt = 0; mt < 5; ++mt) acc[j][mt] = mfma(Q[mt][3 + part], xf[3 + part][j], acc[j][mt]);
        });

        if (mp.isC) {
            const unsigned fo = O_WFQ + ((64 * cg + 2 * n) * 512 + 16 * rg + 4 * w + q) * 16u;
#pragma unroll
            for (int it = 0; it < 2; ++it) {
                u32x4 o0, o1;
#pragma unroll
                for (int jj = 0; jj < 4; ++jj) {
                    o0[jj] = __builtin_amdgcn_perm(fi[it][jj], fr[it][jj], 0x05040100u);
                    o1[jj] = __builtin_amdgcn_perm(fi[it][jj], fr[it][jj], 0x07060302u);
                }
                cst16(rws, (it == 0 && cg == 0 && n == 0) ? 0xfffffff0u : fo + it * 32 * 8192u, o0);
                cst16(rws, fo + it * 32 * 8192u + 8192u, o1);
            }
            if (cg == 0 && w == 1)
                cst4(rws, O_WFQ + (16 * rg + (lane >> 2)) * 16u + (lane & 3) * 4u, pk((blo(ny[0]) + blo(ny[1])) * 0.5f, (blo(ny[2]) - blo(ny[3])) * 0.5f));
        }
#pragma unroll
        for (int mt = 0; mt < 5; ++mt) {
            acc[0][mt] *= AMP;
            if (mt == 0 && mp.ct == 0 && q == 0) { acc[0][0][0] *= AMP0 / AMP; acc[0][0][1] *= AMP0 / AMP; }
        }
        store_xh(acc[0], rws, O_XH0, O_ST0, mp.rb, mp.ct, w, lane, scr);

    }
    const u64 ep = rfl64(ep_in) + 1;
    {
        MAP;
        PIN(pf_code);
        publish(p.ws, PH_P0, mp.rb * 8 + mp.ct, ep);

        if (mp.isC) phase_c<0>(p, mp.g, mp.m, ep, smem, scr);
        else phase_aux(p, Q, mp.ct, mp.aj, mp.m, ep, smem);
    }

    u32x4 W[5][12], QT[5][5], xg[2][5][2];
    {
        MAP; IDS;
        wait_flags(p.ws, PH_A2, mp.ct * 12 + 3 * w + (lane % 3), ep);

        const unsigned wo = O_W2F + ((5 * mp.ct * 48 + 12 * w) * 64 + lane) * 16u;
        phase_g1<0>(p, mp.tt, mp.m, mp.rb, W, wo, xg, ep, smem);
        phase_g2(p, mp.rb, mp.ct, W, wo, ep, scr);
    }

    {
        MAP;
        if (mp.isC) phase_c<1>(p, mp.g, mp.m, ep, smem, scr);
        else {
            phase_a3(p, mp.ct * 12 + mp.aj, ep, scr);
            const int lane = fresh_tid() & 63;
            wait_flags(p.ws, PH_A3, a3_idx(lane), ep);
            wait_flags(p.ws, PH_A3, a3_idx2(lane), ep);
        }
    }
    {
        MAP; IDS;
        const rsrc_t rws = mkrs(p.ws, WS_USED);
#pragma unroll
        for (int kk = 0; kk < 5; ++kk)
#pragma unroll
            for (int mt = 0; mt < 5; ++mt) QT[mt][kk] = cld16(rws, O_QT + (((5 * mp.ct + mt) * 20 + 5 * w + kk) * 64 + lane) * 16u);
        SB();

        const unsigned wo = O_W2T + ((5 * mp.ct * 48 + 12 * w) * 64 + lane) * 16u;
        phase_g1<1>(p, mp.tt, mp.m, mp.rb, W, wo, xg, ep, smem);
        phase_g4(p, mp.rb, mp.ct, W, wo, QT, xg, ep, scr);
    }
    if (blockIdx.x == 0 && threadIdx.x == 0) __hip_atomic_store(epw, ep, __ATOMIC_RELAXED, __HIP_MEMORY_SCOPE_AGENT);

    asm volatile("kall_pf_end:");
}

extern "C" void kernel_launch(void* const* d_in, const int* in_sizes, int n_in, void* d_out, int out_size, void* d_ws, size_t ws_size,
                              hipStream_t stream) {
    Params p;
    p.x = (const uint16_t*)d_in[0]; p.wr = (const uint16_t*)d_in[1]; p.wi = (const uint16_t*)d_in[2];
    p.w1 = (const uint16_t*)d_in[4]; p.b1 = (const uint16_t*)d_in[5]; p.w2 = (const uint16_t*)d_in[6]; p.b2 = (const uint16_t*)d_in[7];
    p.out = (uint16_t*)d_out; p.ws = (char*)d_ws;
    k_all<<<256, 256, 0, stream>>>(p);
}
```

```cpp
#include <hip/hip_runtime.h>
#include <stdint.h>

#pragma clang diagnostic ignored "-Winline-asm"
typedef float f32x4 __attribute__((ext_vector_type(4)));
typedef float f32x2 __attribute__((ext_vector_type(2)));
typedef unsigned u32x4 __attribute__((ext_vector_type(4)));
typedef unsigned u32x2 __attribute__((ext_vector_type(2)));
typedef __bf16 bf16x8 __attribute__((ext_vector_type(8)));
typedef __bf16 bf16x2 __attribute__((ext_vector_type(2)));
typedef __amdgpu_buffer_rsrc_t rsrc_t;
typedef unsigned long long u64;
#define DI __device__ __forceinline__
#define GAS __attribute__((address_space(1)))
#define LAS __attribute__((address_space(3)))

constexpr int S = 2048, D = 640, F = 1536;
#define GSPAD 0
constexpr unsigned GS = 16384u + GSPAD;
constexpr unsigned XH_BYTES = 160u * GS;
constexpr unsigned O_XH0 = 0, O_XH1 = XH_BYTES, O_XH2 = 2 * XH_BYTES, O_XH3 = 3 * XH_BYTES;
constexpr unsigned O_HF1 = 0x0B00000, O_HF2 = 0x1100000;
constexpr unsigned O_W1F = 0x1700000;
constexpr unsigned O_W2F = 0x1900000;
constexpr unsigned O_W2T = 0x1B00000;
constexpr unsigned O_WFQ = 0x1D00000;
constexpr unsigned O_ST0 = 0x1F80000, O_ST2 = 0x1F90000;
constexpr unsigned O_FLG = 0x1FA0000;
constexpr unsigned O_EPO = 0x1FF0000;
constexpr unsigned O_QT = 0x2000000;
constexpr unsigned O_B2P = 0x20D0000;
constexpr unsigned WS_USED = 0x2100000;
constexpr unsigned LDS_SCR = 122880;
enum { PH_P0 = 0, PH_A1, PH_A2, PH_A3, PH_C1, PH_G1A, PH_G2, PH_C2, PH_G1B };

struct Params {
    const uint16_t* x; const uint16_t* wr; const uint16_t* wi; const uint16_t* w1; const uint16_t* b1;
    const uint16_t* w2; const uint16_t* b2; uint16_t* out; char* ws;
};

#define PIN(x) asm volatile("" : "+v"(x))

DI rsrc_t mkrs(const void* p, unsigned bytes) { return __builtin_amdgcn_make_buffer_rsrc((void*)p, 0, bytes, 0x00020000); }
DI u32x4 ld16(rsrc_t r, unsigned off) { return __builtin_amdgcn_raw_buffer_load_b128(r, off, 0, 0); }
DI u32x2 ld8(rsrc_t r, unsigned off) { return __builtin_amdgcn_raw_buffer_load_b64(r, off, 0, 0); }
DI unsigned ld4(rsrc_t r, unsigned off) { return __builtin_amdgcn_raw_buffer_load_b32(r, off, 0, 0); }
DI unsigned ld2(rsrc_t r, unsigned off) { return __builtin_amdgcn_raw_buffer_load_b16(r, off, 0, 0); }
DI void st16(rsrc_t r, unsigned off, u32x4 v) { __builtin_amdgcn_raw_buffer_store_b128(v, r, off, 0, 0); }
DI void st8(rsrc_t r, unsigned off, u32x2 v) { __builtin_amdgcn_raw_buffer_store_b64(v, r, off, 0, 0); }
DI u32x4 cld16(rsrc_t r, unsigned off) { return __builtin_amdgcn_raw_buffer_load_b128(r, off, 0, 16); }
DI u32x2 cld8(rsrc_t r, unsigned off) { return __builtin_amdgcn_raw_buffer_load_b64(r, off, 0, 16); }
DI void cst16(rsrc_t r, unsigned off, u32x4 v) { __builtin_amdgcn_raw_buffer_store_b128(v, r, off, 0, 16); }
DI void cst8(rsrc_t r, unsigned off, u32x2 v) { __builtin_amdgcn_raw_buffer_store_b64(v, r, off, 0, 16); }
DI void cst4(rsrc_t r, unsigned off, unsigned v) { __builtin_amdgcn_raw_buffer_store_b32(v, r, off, 0, 16); }
DI void dma16(rsrc_t r, unsigned voff, unsigned soff, unsigned lds) {
    asm volatile("s_mov_b32 m0, %0\n\ts_nop 0\n\tbuffer_load_dwordx4 %1, %2, %3 offen lds" :: "s"(lds), "v"(voff), "s"(r), "s"(soff) : "m0", "memory");
}
DI void cdma16(rsrc_t r, unsigned voff, unsigned soff, unsigned lds) {
    asm volatile("s_mov_b32 m0, %0\n\ts_nop 0\n\tbuffer_load_dwordx4 %1, %2, %3 offen sc1 lds" :: "s"(lds), "v"(voff), "s"(r), "s"(soff) : "m0", "memory");
}
#define VMWAIT(n) asm volatile("s_waitcnt vmcnt(" #n ")" ::: "memory")
#define VMBAR(n) asm volatile("s_waitcnt vmcnt(" #n ")\n\ts_barrier" ::: "memory")
#define VMLBAR(n) asm volatile("s_waitcnt vmcnt(" #n ") lgkmcnt(0)\n\ts_barrier" ::: "memory")
typedef short s16x4 __attribute__((ext_vector_type(4)));
DI u32x2 trd(const char* a) { return __builtin_bit_cast(u32x2, __builtin_amdgcn_ds_read_tr16_b64_v4i16((LAS s16x4*)a)); }
DI f32x4 mfma(u32x4 a, u32x4 b, f32x4 c) {
    return __builtin_amdgcn_mfma_f32_16x16x32_bf16(__builtin_bit_cast(bf16x8, a), __builtin_bit_cast(bf16x8, b), c, 0, 0, 0);
}
DI unsigned pk(float lo, float hi) { bf16x2 v = __builtin_convertvector((f32x2){lo, hi}, bf16x2); return __builtin_bit_cast(unsigned, v); }
DI float blo(unsigned u) { return __uint_as_float(u << 16); }
DI float bhi(unsigned u) { return __uint_as_float(u & 0xffff0000u); }
DI float dot2(unsigned a, unsigned b, float c) { return __builtin_amdgcn_fdot2_f32_bf16(__builtin_bit_cast(bf16x2, a), __builtin_bit_cast(bf16x2, b), c, false); }
DI float vcos(float r) { return __builtin_amdgcn_cosf(r); }
DI float vsin(float r) { return __builtin_amdgcn_sinf(r); }
DI float fract(float r) { return __builtin_amdgcn_fractf(r); }
DI void bar() { asm volatile("s_waitcnt lgkmcnt(0)\n\ts_barrier" ::: "memory"); }
DI float sumq(float v) {
    auto a = __builtin_amdgcn_permlane16_swap(__float_as_uint(v), __float_as_uint(v), false, false);
    v = __uint_as_float(a[0]) + __uint_as_float(a[1]);
    auto b = __builtin_amdgcn_permlane32_swap(__float_as_uint(v), __float_as_uint(v), false, false);
    return __uint_as_float(b[0]) + __uint_as_float(b[1]);
}
#define SB() __builtin_amdgcn_sched_barrier(0)
DI int fresh_tid() { int t = threadIdx.x; asm volatile("" : "+v"(t)); return t; }
DI unsigned rfl(unsigned v) { return (unsigned)__builtin_amdgcn_readfirstlane((int)v); }
DI u64 rfl64(u64 v) { return (u64)rfl((unsigned)(v >> 32)) << 32 | (u64)rfl((unsigned)v); }
DI int fresh_s(int v) { asm volatile("" : "+s"(v)); return v; }
#define IDS const int tid = fresh_tid(), w = __builtin_amdgcn_readfirstlane(tid >> 6), lane = tid & 63, n = lane & 15, q = lane >> 4

DI GAS u64* flag_ptr(char* ws, int e) { return (GAS u64*)(ws + O_FLG + e * 128); }
DI void publish(char* ws, int ph, int idx, u64 ep) {
    asm volatile("s_waitcnt vmcnt(0)" ::: "memory");
    bar();
    if (threadIdx.x == 0) __hip_atomic_store(flag_ptr(ws, ph * 256 + idx), ep, __ATOMIC_RELAXED, __HIP_MEMORY_SCOPE_AGENT);
}
DI void wait_flags(char* ws, int ph, int idx, u64 ep) {
    GAS u64* f = flag_ptr(ws, ph * 256 + idx);
    for (int s = 0; s < 400000; ++s) {
        const u64 v = __hip_atomic_load(f, __ATOMIC_RELAXED, __HIP_MEMORY_SCOPE_AGENT);
        if (__all(v == ep)) break;
        __builtin_amdgcn_s_sleep(1);
    }
    asm volatile("" ::: "memory");
}

DI u64 peek_flag(char* ws, int ph, int idx) {
    return __hip_atomic_load(flag_ptr(ws, ph * 256 + idx), __ATOMIC_RELAXED, __HIP_MEMORY_SCOPE_AGENT);
}
DI void finish_wait(char* ws, int ph, int idx, u64 ep, u64 first) {
    if (!__all(first == ep)) wait_flags(ws, ph, idx, ep);
    asm volatile("" ::: "memory");
}

constexpr float AMP = 0.05590169943749474f;
constexpr float AMP0 = 0.03952847075210474f;

struct Map { int tt, m, rb, ct, isC, g, ar; };
__host__ __device__ inline Map get_map(int id) {
    Map r;
    const int xcd = id & 7, slot = id >> 3;
    r.tt = xcd + 8 * (slot >> 4); r.m = slot & 15; r.rb = 2 * r.tt + (r.m >> 3); r.ct = r.m & 7;
    const bool aux = slot >= 24 || (slot >= 16 && ((slot >> 2) & 1) == (xcd & 1));
    r.isC = !aux;
    r.g = 20 * xcd + (slot < 16 ? slot : 16 + (slot & 3));
    r.ar = 12 * xcd + (slot >= 24 ? slot - 24 : 8 + (slot & 3));
    return r;
}

template <class H>
DI void gen_q(u32x4 (&Q)[2][20], int cg, int w, int lane, H hook) {
    const int n = lane & 15, q = lane >> 4;
    f32x2 dl, r, d32, c2;
#pragma unroll
    for (int mt = 0; mt < 2; ++mt) {
        const int c = 128 * cg + 32 * w + 16 * mt + n;
        const float pf = c == 1 ? 320.f : (float)(c >> 1);
        const float ph = ((c & 1) && c != 1) ? 0.25f : 0.f;
        dl[mt] = pf * (1.f / 640.f);
        const float t = pf * (float)(128 * cg + 8 * q);
        const float k = floorf(t * (1.f / 640.f));
        r[mt] = __builtin_fmaf(-k, 640.f, t) * (1.f / 640.f) + ph;
        d32[mt] = fract(pf * 0.05f);
        c2[mt] = 2.f * vcos(dl[mt]);
    }
#pragma unroll
    for (int kk = 0; kk < 20; ++kk) {
        hook(kk);
        const f32x2 r1 = r + dl;
        f32x2 v[8];
        v[0] = (f32x2){vcos(r[0]), vcos(r[1])};
        v[1] = (f32x2){vcos(r1[0]), vcos(r1[1])};
#pragma unroll
        for (int j = 2; j < 8; ++j) v[j] = __builtin_elementwise_fma(c2, v[j - 1], -v[j - 2]);
#pragma unroll
        for (int mt = 0; mt < 2; ++mt)
            Q[mt][kk] = (u32x4){pk(v[0][mt], v[1][mt]), pk(v[2][mt], v[3][mt]), pk(v[4][mt], v[5][mt]), pk(v[6][mt], v[7][mt])};
        r = r + d32;
        r = (f32x2){fract(r[0]), fract(r[1])};
        PIN(Q[0][kk]); PIN(Q[1][kk]);
        SB();
    }
}

template <int NA, int WS, class MM>
DI void ktail(f32x4 (&acc)[4][NA], f32x4* sb, int w, int lane, MM mm) {
    f32x4* wr = sb + w * WS + lane;
    f32x4* rd = sb + (w ^ 2) * WS + lane;
    const f32x4* rd2 = sb + (w ^ 3) * WS + NA * 64 + lane;
    mm(2, 0); mm(2, 1); mm(3, 0); mm(3, 1);
    SB();
    mm(1, 0);
#pragma unroll
    for (int m = 0; m < NA; ++m) wr[m * 64] = acc[2][m];
    SB();
    mm(1, 1);
#pragma unroll
    for (int m = 0; m < NA; ++m) wr[(NA + m) * 64] = acc[3][m];
    SB();
    bar();
    f32x4 in1[NA], in0[NA];
#pragma unroll
    for (int m = 0; m < NA; ++m) in1[m] = rd[(NA + m) * 64];
    SB();
    mm(0, 0);
    SB();
#pragma unroll
    for (int m = 0; m < NA; ++m) rd[(NA + m) * 64] = acc[1][m] + in1[m];
    SB();
#pragma unroll
    for (int m = 0; m < NA; ++m) in0[m] = rd[m * 64];
    mm(0, 1);
    SB();
    bar();
#pragma unroll
    for (int m = 0; m < NA; ++m) in1[m] = rd2[m * 64];
#pragma unroll
    for (int m = 0; m < NA; ++m) acc[0][m] += in0[m];
    SB();
#pragma unroll
    for (int m = 0; m < NA; ++m) acc[0][m] += in1[m];
}

DI void store_xh(const f32x4 (&v)[5], rsrc_t rws, unsigned o_xh, unsigned o_st, int rb, int ct, int t, int lane, char* sb) {
    const int n = lane & 15, q = lane >> 4;
    u32x2 d[5];
    float ss = 0.f;
#pragma unroll
    for (int mt = 0; mt < 5; ++mt) {
        d[mt] = (u32x2){pk(v[mt][0], v[mt][1]), pk(v[mt][2], v[mt][3])};
        const unsigned m0 = (mt == 0 && ct == 0 && q == 0) ? (d[mt][0] & 0xffff0000u) : d[mt][0];
        ss = dot2(m0, m0, ss); ss = dot2(d[mt][1], d[mt][1], ss);
    }
    u32x2* mine = (u32x2*)(sb + (t ^ 2) * 10240) + lane;
    const u32x2* other = (const u32x2*)(sb + (t ^ 3) * 10240) + lane;
    const unsigned base = o_xh + (20 * ct + q) * GS + (2 * rb + (t >> 1)) * 256u + n * 16u;
    if (t & 1) {
        mine[0] = d[0]; mine[64] = d[1]; mine[128] = d[2];
        ss = sumq(ss);
        mine[192] = (u32x2){__float_as_uint(ss), 0u};
        bar();
        const u32x2 a = other[0], b = other[64];
        cst16(rws, base + 12 * GS, (u32x4){a[0], a[1], d[3][0], d[3][1]});
        cst16(rws, base + 16 * GS, (u32x4){b[0], b[1], d[4][0], d[4][1]});
    } else {
        mine[0] = d[3]; mine[64] = d[4];
        ss = sumq(ss);
        bar();
        const u32x2 a = other[0], b = other[64], c = other[128], s1 = other[192];
        cst16(rws, base, (u32x4){d[0][0], d[0][1], a[0], a[1]});
        cst16(rws, base + 4 * GS, (u32x4){d[1][0], d[1][1], b[0], b[1]});
        cst16(rws, base + 8 * GS, (u32x4){d[2][0], d[2][1], c[0], c[1]});
        const unsigned ln = ((t >> 1) + 2 * ((rb >> 2) & 1)) * 16 + n;
        const unsigned so = o_st + ((ct * 4 + (rb >> 3)) * 64 + ln) * 16u + (rb & 3) * 4u;
        cst4(rws, q == 0 ? so : 0xfffffff0u, pk(ss, __uint_as_float(s1[0])));
    }
}

DI int w1f_idx(int m, int lane) { return 5 * m + lane % 5; }
DI void w1f_dma(rsrc_t rws, unsigned lb, int m, int w, int lane, int i) {
    cdma16(rws, lane * 16u, O_W1F + m * 122880u + w * 30720u + i * 1024u, lb + w * 30720u + i * 1024u);
}
DI int a3_idx(int lane) { return lane; }
DI int a3_idx2(int lane) { return 64 + (lane & 31); }

DI unsigned rot5(int a, int b) { const int s = a + b; return s >= 5 ? s - 5 : s; }

DI void phase_p0(const Params& p, int g, char* smem) {
    IDS;
    const int rb = g / 5, cg = g - 5 * rb;
    const rsrc_t rws = mkrs(p.ws, WS_USED), rx = mkrs(p.x, S * D * 2);
    const unsigned lb = (unsigned)(size_t)(LAS char*)smem;
    const unsigned sw = q * 1280u + ((n ^ q) << 4);
    const rsrc_t rwr = mkrs(p.wr, S * 321 * 2), rwi = mkrs(p.wi, S * 321 * 2);
    const int ks0 = 16 * (rb >> 4) + 32 * (16 * ((rb >> 2) & 3) + 4 * (rb & 3));
    unsigned fr[2][4], fi[2][4], ny[4];
    const unsigned o1 = ((ks0 + 4 * w + q) * 321 + 64 * cg + 2 * n) * 2u;
    const bool nyq = cg == 0 && w == 1;
    const int ks = ks0 + (lane >> 2) + 32 * (lane & 3), kn = (2048 - ks) & 2047;
    const unsigned oa = nyq ? (ks * 321 + 320) * 2u : 0x80000000u, ob = nyq ? (kn * 321 + 320) * 2u : 0x80000000u;
    u32x4 Q[2][20];
    gen_q(Q, cg, w, lane, [&](int kk) {
        if (kk < 10) {
#pragma unroll
            for (int i = 2 * kk; i < 2 * kk + 2; ++i)
                dma16(rx, sw ^ ((i & 3) << 6), (64 * rb + 16 * w + 4 * (i & 3)) * 1280u + rot5(cg, i >> 2) * 256u, lb + (i >> 2) * 16384u + w * 4096u + (i & 3) * 1024u);
        } else if (kk < 18) {
            const int it = (kk - 10) >> 2, jj = (kk - 10) & 3;
            fr[it][jj] = __builtin_amdgcn_raw_buffer_load_b32(rwr, o1 + it * 64, jj * 32 * 321 * 2, 0);
            fi[it][jj] = __builtin_amdgcn_raw_buffer_load_b32(rwi, o1 + it * 64, jj * 32 * 321 * 2, 0);
        } else if (kk == 18) {
            ny[0] = ld2(rwr, oa); ny[1] = ld2(rwr, ob);
        } else {
            ny[2] = ld2(rwi, oa); ny[3] = ld2(rwi, ob);
        }
    });

    const f32x4 z = {0.f, 0.f, 0.f, 0.f};
    f32x4 acc[2][4];
    f32x4 gr = z, sr = z;
    unsigned one = 0x3f803f80u;
    PIN(one);
    const u32x4 ones = {one, one, one, one};
    const unsigned rb0 = n * 256u + ((n ^ q) << 4);
    auto rd = [&](int kk, int j) -> u32x4 { return *(const u32x4*)(smem + (rb0 ^ ((kk & 3) << 6)) + (kk >> 2) * 16384 + j * 4096); };
    auto rdo = [&](int kk) -> u32x4 { return *(const u32x4*)(smem + ((rb0 ^ ((kk & 3) << 6)) + w * 4096u) + (kk >> 2) * 16384); };
    VMBAR(36);

    u32x4 xa[2][4], xo[2];
#pragma unroll
    for (int j = 0; j < 4; ++j) xa[0][j] = rd(0, j);
    xo[0] = rdo(0);
    SB();
#pragma unroll
    for (int kk = 0; kk < 20; ++kk) {
        if (kk == 3) { VMBAR(32); }
        if (kk == 7) { VMBAR(28); }
        if (kk == 11) {

#pragma unroll
            for (int i = 0; i < 4; ++i) { PIN(ny[i]); PIN(fr[0][i]); PIN(fr[1][i]); PIN(fi[0][i]); PIN(fi[1][i]); }
            const unsigned fo = O_WFQ + ((64 * cg + 2 * n) * 512 + 16 * rb + 4 * w + q) * 16u;
#pragma unroll
            for (int it = 0; it < 2; ++it) {
                u32x4 o0, o1;
#pragma unroll
                for (int jj = 0; jj < 4; ++jj) {
                    o0[jj] = __builtin_amdgcn_perm(fi[it][jj], fr[it][jj], 0x05040100u);
                    o1[jj] = __builtin_amdgcn_perm(fi[it][jj], fr[it][jj], 0x07060302u);
                }
                cst16(rws, (it == 0 && cg == 0 && n == 0) ? 0xfffffff0u : fo + it * 32 * 8192u, o0);
                cst16(rws, fo + it * 32 * 8192u + 8192u, o1);
            }
            cst4(rws, nyq ? O_WFQ + (16 * rb + (lane >> 2)) * 16u + (lane & 3) * 4u : 0xfffffff0u,
                 pk((blo(ny[0]) + blo(ny[1])) * 0.5f, (blo(ny[2]) - blo(ny[3])) * 0.5f));
            SB();

            asm volatile("s_barrier" ::: "memory");

        }
        SB();
#pragma unroll
        for (int j = 0; j < 4; ++j) {
            acc[0][j] = mfma(Q[0][kk], xa[kk & 1][j], kk == 0 ? z : acc[0][j]);
            if (kk < 19) xa[(kk + 1) & 1][j] = rd(kk + 1, j);
            if (j == 1) gr = mfma(xo[kk & 1], xo[kk & 1], gr);
            if (j == 3) sr = mfma(ones, xo[kk & 1], sr);
            acc[1][j] = mfma(Q[1][kk], xa[kk & 1][j], kk == 0 ? z : acc[1][j]);
            if (kk < 19 && j == 0) xo[(kk + 1) & 1] = rdo(kk + 1);
            SB();
        }
    }

    const bool dc = cg == 0 && w == 0 && q == 0;
#pragma unroll
    for (int mt = 0; mt < 2; ++mt)
#pragma unroll
        for (int b = 0; b < 2; ++b) {
            f32x4 v0 = acc[mt][2 * b] * AMP, v1 = acc[mt][2 * b + 1] * AMP;
            if (mt == 0 && dc) { v0[0] *= AMP0 / AMP; v0[1] *= AMP0 / AMP; v1[0] *= AMP0 / AMP; v1[1] *= AMP0 / AMP; }
            cst16(rws, O_XH0 + (32 * cg + 8 * w + 4 * mt + q) * GS + (2 * rb + b) * 256u + n * 16u,
                  (u32x4){pk(v0[0], v0[1]), pk(v0[2], v0[3]), pk(v1[0], v1[1]), pk(v1[2], v1[3])});
        }
    {
        const int r = n & 3;
        const float dg = r == 0 ? gr[0] : r == 1 ? gr[1] : r == 2 ? gr[2] : gr[3];
        const float ss = sumq(q == (n >> 2) ? dg : 0.f), sm = sr[0];
        const float var = fmaxf(__builtin_fmaf(-sm * (1.f / 640.f), sm, ss) * (1.f / 640.f), 0.f);
        const unsigned so = O_ST0 + (((rb >> 3) * 64 + (2 * ((rb >> 2) & 1) + (w >> 1)) * 16 + n) * 2 + (w & 1)) * 16u + (rb & 3) * 4u;
        cst4(rws, (q == 0 && cg == 0) ? so : 0xfffffff0u, __float_as_uint(__builtin_amdgcn_rsqf(var + 1e-5f)));
    }

}

DI void phase_aux(const Params& p, int ar, int m, u64 ep_in, u64& ep, char* smem) {
    IDS;
    const int xcd = ar / 12, a = ar - 12 * xcd;
    const bool act = a < 10;
    const int spare = 2 * xcd + a - 10;
    const int fj = 2 * xcd + (a >= 5), cg = act ? (a >= 5 ? a - 5 : a) : spare;
    const rsrc_t rws = mkrs(p.ws, WS_USED);
    const unsigned lb = (unsigned)(size_t)(LAS char*)smem;
    const f32x4 z = {0.f, 0.f, 0.f, 0.f};
    if (act || spare < 5) {
        const rsrc_t rw1 = mkrs(p.w1, act ? D * F * 2 : 0), rw2 = mkrs(p.w2, act ? D * F * 2 : 0);
        unsigned v1[3];
#pragma unroll
        for (int b = 0; b < 3; ++b) {
            const int ix = 64 * b + lane, rl = ix / 12, cp = ix - 12 * rl;
            v1[b] = rl * 3072u + ((cp ^ ((rl >> 3) << 1)) << 4);
        }
        auto dma1 = [&](int i) {
            const int k = i / 6, u = (i % 6) / 3, b = i % 3;
            dma16(rw1, v1[b], (128 * rot5(cg, k) + 16 * (2 * w + u)) * 3072u + fj * 192u, lb + k * 24576u + (2 * w + u) * 3072u + b * 1024u);
        };
        const unsigned sw = q * 1280u + ((n ^ q) << 4);
        auto dma2 = [&](int s, int t) {
            dma16(rw2, sw ^ (((2 * w + t) & 3) << 6), (96 * fj + 24 * w + 4 * t) * 1280u + rot5(cg, s) * 256u, lb + (s ? (s - 1) * 24576u : 122880u) + w * 6144u + t * 1024u);
        };
        u32x4 Q[2][20];
        gen_q(Q, cg, w, lane, [&](int kk) {
            const int i0 = kk < 16 ? 2 * kk : 16 + kk, i1 = kk < 16 ? 2 * kk + 2 : 17 + kk;
#pragma unroll
            for (int i = i0; i < i1; ++i) { if (i < 30) dma1(i); else dma2(0, i - 30); }
        });

        if (act) {
            const int qq = (lane >> 2) & 3, pp = lane & 3;
            const unsigned tb = (8 * q + qq) * 192u + (pp >> 1) * 16u + (pp & 1) * 8u;
            const unsigned tbe = tb + (q & 1) * 32u, tbo = tb - (q & 1) * 32u;
            auto rd1 = [&](int kk, int ft) -> u32x4 {
                const char* ap = smem + ((ft & 1) ? tbo : tbe) + (kk >> 2) * 24576u;
                const u32x2 lo = trd(ap + (kk & 3) * 6144 + ft * 32), hi = trd(ap + (kk & 3) * 6144 + ft * 32 + 768);
                return (u32x4){lo[0], lo[1], hi[0], hi[1]};
            };
            const unsigned rb0 = n * 256u + ((n ^ q) << 4);
            auto rd2 = [&](int kk, int ft) -> u32x4 {
                const int s = kk >> 2;
                return *(const u32x4*)(smem + ((rb0 ^ ((kk & 3) << 6)) + (s ? (s - 1) * 24576u : 122880u)) + ft * 4096);
            };
            f32x4 acc[2][6];
            u32x4 fa[2][6];
            VMBAR(30);

#pragma unroll
            for (int ft = 0; ft < 6; ++ft) fa[0][ft] = rd1(0, ft);
            SB();
#pragma unroll
            for (int kk = 0; kk < 20; ++kk) {
                if ((kk & 3) == 3) { VMLBAR(24); }
                SB();
#pragma unroll
                for (int ft = 0; ft < 6; ++ft) {
                    acc[0][ft] = mfma(Q[0][kk], fa[kk & 1][ft], kk == 0 ? z : acc[0][ft]);
                    fa[(kk + 1) & 1][ft] = kk < 19 ? rd1(kk + 1, ft) : rd2(0, ft);
                    acc[1][ft] = mfma(Q[1][kk], fa[kk & 1][ft], kk == 0 ? z : acc[1][ft]);
                    if ((kk & 3) == 3 && kk < 19) dma2((kk >> 2) + 1, ft);
                    SB();
                }
            }

            {
                const bool dc = cg == 0 && w == 0 && q == 0;
#pragma unroll
                for (int ft = 0; ft < 6; ++ft) {
                    f32x4 a0 = acc[0][ft] * AMP;
                    const f32x4 a1 = acc[1][ft] * AMP;
                    if (dc) { a0[0] = 0.f; a0[1] *= AMP0 / AMP; }
                    cst16(rws, O_W1F + (((6 * fj + ft) * 20 + 4 * cg + w) * 64 + lane) * 16u, (u32x4){pk(a0[0], a0[1]), pk(a0[2], a0[3]), pk(a1[0], a1[1]), pk(a1[2], a1[3])});
                }
            }
            SB();

#pragma unroll
            for (int kk = 0; kk < 20; ++kk) {
                if (kk == 3) VMBAR(24);
                if (kk == 7) { VMWAIT(0); ep = rfl64(ep_in) + 1; publish(p.ws, PH_A1, 5 * fj + cg, ep); }
                SB();
#pragma unroll
                for (int ft = 0; ft < 6; ++ft) {
                    acc[0][ft] = mfma(fa[kk & 1][ft], Q[0][kk], kk == 0 ? z : acc[0][ft]);
                    acc[1][ft] = mfma(fa[kk & 1][ft], Q[1][kk], kk == 0 ? z : acc[1][ft]);
                    if (kk < 19) fa[(kk + 1) & 1][ft] = rd2(kk + 1, ft);
                    SB();
                }
            }

#pragma unroll
            for (int mt = 0; mt < 2; ++mt) {
                const float am = (mt == 0 && cg == 0 && w == 0 && n < 2) ? AMP0 : AMP;
#pragma unroll
                for (int u = 0; u < 3; ++u) {
                    const f32x4 v0 = acc[mt][2 * u] * am, v1_ = acc[mt][2 * u + 1] * am;
                    cst16(rws, O_W2F + (((8 * cg + 2 * w + mt) * 48 + 3 * fj + u) * 64 + lane) * 16u,
                          (u32x4){pk(v0[0], v0[1]), pk(v0[2], v0[3]), pk(v1_[0], v1_[1]), pk(v1_[2], v1_[3])});
                }
            }
            publish(p.ws, PH_A2, 5 * fj + cg, ep);
        } else {
            const rsrc_t rb2 = mkrs(p.b2, D * 2);
            f32x4 a0 = z, a1 = z;
#pragma unroll
            for (int kk = 0; kk < 20; ++kk) {
                const u32x4 bf = ld16(rb2, (128 * rot5(cg, kk >> 2) + 32 * (kk & 3) + 8 * q) * 2u);
                a0 = mfma(bf, Q[0][kk], a0); a1 = mfma(bf, Q[1][kk], a1);
            }
            const float am = (cg == 0 && w == 0 && n < 2) ? AMP0 : AMP;
            cst4(rws, q < 2 ? O_B2P + (128 * cg + 32 * w + 16 * q + n) * 4u : 0xfffffff0u, __float_as_uint(q == 0 ? a0[0] * am : a1[0] * AMP));
            ep = rfl64(ep_in) + 1;
            publish(p.ws, PH_A2, 80 + cg, ep);
        }
    } else ep = rfl64(ep_in) + 1;

    wait_flags(p.ws, PH_A1, w1f_idx(m, lane), ep);

#pragma unroll
    for (int i = 0; i < 30; ++i) w1f_dma(rws, lb, m, w, lane, i);
    VMWAIT(0);
}

DI void phase_a3(const Params& p, int ar, u64 ep, char* scr) {
    IDS;
    const rsrc_t rw2 = mkrs(p.w2, D * F * 2), rws = mkrs(p.ws, WS_USED);
    const int u = ar >> 1, half = ar & 1;
    const unsigned lb = (unsigned)(size_t)(LAS char*)scr;
#pragma unroll
    for (int i = 0; i < 5; ++i) {
        const unsigned o = (w * 5 + i) * 1024u + lane * 16u, row = o / 640u;
        dma16(rw2, (32 * u + row) * 1280u + half * 640u + (o - row * 640u), 0u, lb + (w * 5 + i) * 1024u);
    }
#pragma unroll
    for (int i = 0; i < 3; ++i) {
        const int fi = ar * 4 + w + 384 * i;
        if (fi < 800) {
            const int T = fi / 20, kk = fi - 20 * T;
            const float df = (float)(16 * T + n);
            u32x4 v;
#pragma unroll
            for (int hh = 0; hh < 2; ++hh)
#pragma unroll
                for (int pr = 0; pr < 2; ++pr) {
                    const int c = 32 * kk + 16 * hh + 4 * q + 2 * pr;
                    const float t = (float)(c >> 1) * df;
                    const float k = floorf(t * (1.f / 640.f));
                    const float r = __builtin_fmaf(-k, 640.f, t) * (1.f / 640.f);
                    float v0 = vcos(r) * AMP, v1 = vcos(r + 0.25f) * AMP;
                    if (c == 0) { v0 = AMP0; v1 = (n & 1) ? -AMP0 : AMP0; }
                    v[2 * hh + pr] = pk(v0, v1);
                }
            cst16(rws, O_QT + fi * 1024u + lane * 16u, v);
        }
    }
    asm volatile("s_waitcnt vmcnt(0)" ::: "memory");
    bar();
#pragma unroll
    for (int i = 0; i < 5; ++i) {
        const int tl = 5 * w + i;
        const char* a = scr + (4 * q + ((lane >> 2) & 3)) * 640 + tl * 32 + (lane & 3) * 8;
        const u32x2 lo = trd(a), hi = trd(a + 16 * 640);
        cst16(rws, O_W2T + (((20 * half + tl) * 48 + u) * 64 + lane) * 16u, (u32x4){lo[0], lo[1], hi[0], hi[1]});
    }
    publish(p.ws, PH_A3, ar, ep);
}

template <int L>
DI void phase_c(const Params& p, int g, int m1, u64 ep, char* lds0, char* smem) {
    constexpr unsigned o_in = L ? O_XH2 : O_XH0, o_st = L ? O_ST2 : O_ST0, o_out = L ? O_XH3 : O_XH1;
    constexpr int SO = L ? 28 : 8;
    IDS;
    const rsrc_t rws = mkrs(p.ws, WS_USED);
    const int c = w >> 1, kh = w & 1;
    u32x4 B1r[2], B1i[2], A3[4][4], A6[2][2];
    f32x2 twc[4][2], tws[4][2];
    u64 fv;
    {
        const float ka = (float)(16 * kh + n);
#pragma unroll
        for (int ks = 0; ks < 2; ++ks)
#pragma unroll
            for (int m = 0; m < 4; ++m) {
                const float r = (float)(16 * ks + 4 * q + m) * (1.f / 32.f) * ka;
                const float cs = vcos(r), sn = vsin(r);
                B1r[ks][m] = pk(cs, sn); B1i[ks][m] = pk(-sn, cs);
            }
#pragma unroll
        for (int bt = 0; bt < 4; ++bt)
#pragma unroll
            for (int r = 0; r < 4; ++r) {
                const float a = (float)(16 * bt + 4 * q + r) * ka * (1.f / 2048.f);
                twc[bt][r >> 1][r & 1] = vcos(a); tws[bt][r >> 1][r & 1] = vsin(a);
            }
        SB();
        fv = peek_flag(p.ws, L ? PH_G2 : PH_P0, L || tid < 160 ? tid : 0);
        SB();
#pragma unroll
        for (int bt = 0; bt < 4; ++bt)
#pragma unroll
            for (int m = 0; m < 4; ++m) {
                const float a = (float)n * (float)(16 * bt + 4 * q + m) * (1.f / 64.f);
                const float cs = vcos(a), sn = vsin(a);
#pragma unroll
                for (int mt = 0; mt < 4; ++mt) {
                    const int k4 = (mt * m) & 3;
                    A3[mt][bt][m] = k4 == 0 ? pk(cs, sn) : k4 == 1 ? pk(-sn, cs) : k4 == 2 ? pk(-cs, -sn) : pk(sn, -cs);
                }
            }
        const float rio = (float)(n & 1) * -0.25f;
#pragma unroll
        for (int rt = 0; rt < 2; ++rt)
#pragma unroll
            for (int ks = 0; ks < 2; ++ks)
#pragma unroll
                for (int m = 0; m < 4; ++m) {
                    const float a = (float)(16 * (w >> 1) + 8 * rt + (n >> 1)) * (float)(16 * ks + 4 * q + m) * (1.f / 32.f) + rio;
                    A6[rt][ks][m] = pk(vcos(a) * (1.f / 2048.f), vcos(a + 0.25f) * (1.f / 2048.f));
                }
#pragma unroll
        for (int i = 0; i < 4; ++i) {
            if (i < 2) { PIN(B1r[i]); PIN(B1i[i]); PIN(A6[i][0]); PIN(A6[i][1]); }
            PIN(A3[i][0]); PIN(A3[i][1]); PIN(A3[i][2]); PIN(A3[i][3]);
            PIN(twc[i][0]); PIN(twc[i][1]); PIN(tws[i][0]); PIN(tws[i][1]);
        }
    }
    SB();

    finish_wait(p.ws, L ? PH_G2 : PH_P0, L || tid < 160 ? tid : 0, ep, fv);
    bar();

    u32x4 xin[4], st[L ? 8 : 2], wf[4];
    {
        const int h = q & 1, hi = q >> 1;
#pragma unroll
        for (int mt = 0; mt < 4; ++mt) wf[mt] = cld16(rws, O_WFQ + ((2 * g + c) * 512 + (kh * 4 + mt) * 64 + lane) * 16u);
        if constexpr (L) {
#pragma unroll
            for (int cp = 0; cp < 8; ++cp) st[cp] = cld16(rws, o_st + ((cp * 4 + w) * 64 + lane) * 16u);
        } else {
#pragma unroll
            for (int hf = 0; hf < 2; ++hf) st[hf] = cld16(rws, o_st + ((w * 64 + lane) * 2 + hf) * 16u);
        }
#pragma unroll
        for (int ii = 0; ii < 4; ++ii) xin[ii] = cld16(rws, o_in + g * GS + (2 * (8 * w + 4 * hi + ii) + h) * 256u + n * 16u);
        if (!L) fv = peek_flag(p.ws, PH_A1, w1f_idx(m1, lane));
    }
    SB();
    u32x4* FR = (u32x4*)smem;
    u32x4* RAW = (u32x4*)(smem + 16384);
    {
        const int h = q & 1, hi = q >> 1;
        float rs[4][2];
        const unsigned sel0 = (unsigned)fresh_s(0x00003f80), sel1 = (unsigned)fresh_s(0x3f800000);
#pragma unroll
        for (int ii = 0; ii < 4; ++ii)
#pragma unroll
            for (int hf = 0; hf < 2; ++hf) {
                if constexpr (L) {
                    float s = 0.f;
#pragma unroll
                    for (int cp = 0; cp < 8; ++cp) s = dot2(st[cp][ii], hf ? sel1 : sel0, s);
                    rs[ii][hf] = __builtin_amdgcn_rsqf(__builtin_fmaf(s, 1.f / 640.f, 1e-5f));
                } else rs[ii][hf] = __uint_as_float(st[hf][ii]);
            }
#pragma unroll
        for (int ii = 0; ii < 4; ++ii) RAW[((8 * w + 4 * hi + ii) * 2 + h) * 16 + n] = xin[ii];
        const int aq = 2 * w + hi;
#pragma unroll
        for (int hf = 0; hf < 2; ++hf)
#pragma unroll
            for (int cc = 0; cc < 2; ++cc) {
                u32x4 o;
#pragma unroll
                for (int ii = 0; ii < 4; ++ii) {
                    const unsigned u = xin[ii][hf * 2 + cc];
                    float re = blo(u) * rs[ii][hf];
                    if (cc == 0 && g == 0) re = 0.f;
                    o[ii] = pk(re, bhi(u) * rs[ii][hf]);
                }
                FR[((cc * 4 + 2 * h + hf) * 2 + (aq >> 2)) * 64 + (aq & 3) * 16 + n] = o;
            }
    }
    bar();

    const unsigned lb = (unsigned)(size_t)(LAS char*)lds0;
    if (!L) finish_wait(p.ws, PH_A1, w1f_idx(m1, lane), ep, fv);
    f32x4 tr[4], ti[4];
    {
        u32x4 a[4][2];
#pragma unroll
        for (int bt = 0; bt < 4; ++bt) { a[bt][0] = FR[((c * 4 + bt) * 2 + 0) * 64 + lane]; a[bt][1] = FR[((c * 4 + bt) * 2 + 1) * 64 + lane]; }
        SB();
#pragma unroll
        for (int bt = 0; bt < 4; ++bt) {
            const f32x4 z = {0.f, 0.f, 0.f, 0.f};
            tr[bt] = mfma(a[bt][1], B1r[1], mfma(a[bt][0], B1r[0], z));
            ti[bt] = mfma(a[bt][1], B1i[1], mfma(a[bt][0], B1i[0], z));
            if (!L) { w1f_dma(rws, lb, m1, w, lane, bt); SB(); }
        }
    }
    bar();
    u32x4 P[4], Pn[4];
#pragma unroll
    for (int bt = 0; bt < 4; ++bt)
#pragma unroll
        for (int r2 = 0; r2 < 2; ++r2) {
            const f32x2 a = {tr[bt][2 * r2], tr[bt][2 * r2 + 1]}, b = {ti[bt][2 * r2], ti[bt][2 * r2 + 1]};
            const f32x2 xr = a * twc[bt][r2] + b * tws[bt][r2], xi = b * twc[bt][r2] - a * tws[bt][r2];
            P[bt][2 * r2] = pk(xr[0], xi[0]); P[bt][2 * r2 + 1] = pk(xr[1], xi[1]);
            Pn[bt][2 * r2] = pk(xi[0], -xr[0]); Pn[bt][2 * r2 + 1] = pk(xi[1], -xr[1]);
            if (!L && r2 == 1) { w1f_dma(rws, lb, m1, w, lane, 4 + bt); SB(); }
        }
    u32x4 R[4], Rn[4];
    u64 fv2 = ep;
#pragma unroll
    for (int mt = 0; mt < 4; ++mt) {
        if (L && mt == 2) { SB(); fv = peek_flag(p.ws, PH_A3, a3_idx(lane)); fv2 = peek_flag(p.ws, PH_A3, a3_idx2(lane)); SB(); }
        f32x4 zr = {0.f, 0.f, 0.f, 0.f}, zi = {0.f, 0.f, 0.f, 0.f};
#pragma unroll
        for (int bt = 0; bt < 4; ++bt) { zr = mfma(A3[mt][bt], P[bt], zr); zi = mfma(A3[mt][bt], Pn[bt], zi); }
        if (!L) { w1f_dma(rws, lb, m1, w, lane, 8 + 2 * mt); w1f_dma(rws, lb, m1, w, lane, 9 + 2 * mt); SB(); }
        unsigned pr[4], pi[4];
#pragma unroll
        for (int r = 0; r < 4; ++r) {
            const float fr = blo(wf[mt][r]), fi = bhi(wf[mt][r]);
            const float yr = zr[r] * fr - zi[r] * fi, yi = zr[r] * fi + zi[r] * fr;
            pr[r] = pk(yr, -yi); pi[r] = pk(yi, yr);
        }
        R[mt] = (u32x4){pr[0], pr[1], pr[2], pr[3]}; Rn[mt] = (u32x4){pi[0], pi[1], pi[2], pi[3]};
    }

    SB();
    if (L) { finish_wait(p.ws, PH_A3, a3_idx(lane), ep, fv); finish_wait(p.ws, PH_A3, a3_idx2(lane), ep, fv2); }
    SB();
    unsigned* TR = (unsigned*)smem;
#pragma unroll
    for (int bt = 0; bt < 4; ++bt) {
        f32x4 ur = {0.f, 0.f, 0.f, 0.f}, ui = {0.f, 0.f, 0.f, 0.f};
#pragma unroll
        for (int kt = 0; kt < 4; ++kt) { ur = mfma(A3[bt][kt], R[kt], ur); ui = mfma(A3[bt][kt], Rn[kt], ui); }
        if constexpr (!L) {
#pragma unroll
            for (int i = 16 + 4 * bt; i < 20 + 4 * bt && i < 30; ++i) w1f_dma(rws, lb, m1, w, lane, i);
            SB();
        }
#pragma unroll
        for (int r2 = 0; r2 < 2; ++r2) {
            const f32x2 a = {ur[2 * r2], ur[2 * r2 + 1]}, b = {ui[2 * r2], ui[2 * r2 + 1]};
            const f32x2 xr = a * twc[bt][r2] - b * tws[bt][r2], xi = b * twc[bt][r2] + a * tws[bt][r2];
#pragma unroll
            for (int e = 0; e < 2; ++e) {
                const int r = 2 * r2 + e;
                const int b_ = 16 * bt + 4 * q + r;
                const int kq = 4 * kh + (n >> 2), f = (2 * q + r2) & 7;
                TR[c * 2048 + b_ * 32 + ((kq ^ f) << 2) + (n & 3)] = pk(xr[e], xi[e]);
            }
        }
        SB();
    }
    bar();

    {
        const int h = w & 1, ah = w >> 1;
        const u32x4* TRv = (const u32x4*)smem;
        u32x4 bq[2][2][2], raw[2][2];
#pragma unroll
        for (int hf = 0; hf < 2; ++hf)
#pragma unroll
            for (int cc = 0; cc < 2; ++cc) {
                const int b_ = 16 * (2 * h + hf) + n, f = (n >> 1) & 7;
                bq[hf][cc][0] = TRv[cc * 512 + b_ * 8 + ((0 + q) ^ f)]; bq[hf][cc][1] = TRv[cc * 512 + b_ * 8 + ((4 + q) ^ f)];
            }
#pragma unroll
        for (int rt = 0; rt < 2; ++rt)
#pragma unroll
            for (int e = 0; e < 2; ++e) raw[rt][e] = RAW[((16 * ah + 8 * rt + 2 * q + e) * 2 + h) * 16 + n];
        SB();
        u32x4 o[2][2];
#pragma unroll
        for (int rt = 0; rt < 2; ++rt) {
#pragma unroll
            for (int hf = 0; hf < 2; ++hf)
#pragma unroll
                for (int cc = 0; cc < 2; ++cc) {
                    const unsigned r0 = raw[rt][0][hf * 2 + cc], r1 = raw[rt][1][hf * 2 + cc];
                    f32x4 y = {0.f, 0.f, 0.f, 0.f};
                    y = mfma(A6[rt][0], bq[hf][cc][0], y); y = mfma(A6[rt][1], bq[hf][cc][1], y);
                    if (cc == 0 && g == 0) { y[0] = 0.f; y[2] = 0.f; }
                    o[rt][0][hf * 2 + cc] = pk(y[0] + blo(r0), y[1] + bhi(r0));
                    o[rt][1][hf * 2 + cc] = pk(y[2] + blo(r1), y[3] + bhi(r1));
                }
#pragma unroll
            for (int e = 0; e < 2; ++e)
                cst16(rws, o_out + g * GS + (2 * (16 * ah + 8 * rt + 2 * q + e) + h) * 256u + n * 16u, o[rt][e]);
        }
    }

    publish(p.ws, L ? PH_C2 : PH_C1, g, ep);

}

template <int L>
DI void phase_g1(const Params& p, int tt, int fs, int rb, int ct, u32x4 (&W)[5][12], unsigned wo, u32x4 (&xg)[2][5][2], u64 ep, char* smem) {
    constexpr unsigned o_in = L ? O_XH3 : O_XH1, o_hf = L ? O_HF2 : O_HF1;
    constexpr int SO = L ? 36 : 17;
    constexpr int DP = 8;
    IDS;
    const rsrc_t rws = mkrs(p.ws, WS_USED), rb1 = mkrs(p.b1, F * 2);
    u32x2 bb[6];
#pragma unroll
    for (int mt = 0; mt < 6; ++mt) bb[mt] = ld8(rb1, (96 * fs + 16 * mt + 4 * q) * 2u);
    {
        int e = (L ? PH_C2 : PH_C1) * 256 + w * 40 + (lane < 40 ? lane : 0);
        if (!L && lane >= 40) {
            const int cg = (lane & 1) ? (80 * ct + 79) >> 7 : (80 * ct) >> 7;
            e = PH_A2 * 256 + (lane < 48 ? 5 * (4 * w + ((lane >> 1) & 3)) + cg : 80 + cg);
        }
        wait_flags(p.ws, 0, e, ep);
    }
    bar();

    const int blk = 4 * tt + w;
    const unsigned xo = o_in + q * GS + blk * 256u + n * 16u;
    const unsigned go = O_XH3 + (40 * w + q) * GS + (2 * rb) * 256u + n * 16u;
    u32x4 l[DP][2];
#pragma unroll
    for (int kk = 0; kk < DP; ++kk)
#pragma unroll
        for (int e = 0; e < 2; ++e) l[kk][e] = cld16(rws, xo + (8 * kk + 4 * e) * GS);
    SB();
    const u32x4* WL = (const u32x4*)smem + lane;
    f32x4 acc[6][2], gr0 = {0.f, 0.f, 0.f, 0.f}, gr1 = {0.f, 0.f, 0.f, 0.f};
#pragma unroll
    for (int mt = 0; mt < 6; ++mt) { acc[mt][0] = (f32x4){0.f, 0.f, 0.f, 0.f}; acc[mt][1] = (f32x4){0.f, 0.f, 0.f, 0.f}; }
    u32x4 A[2][6];
#pragma unroll
    for (int mt = 0; mt < 6; ++mt) A[0][mt] = WL[(mt * 20) * 64];
    u32x4 f0 = {l[0][0][0], l[0][0][1], l[0][1][0], l[0][1][1]}, f1 = {l[0][0][2], l[0][0][3], l[0][1][2], l[0][1][3]};
    if (q == 0) { f0[0] &= 0xffff0000u; f1[0] &= 0xffff0000u; }
    SB();
#pragma unroll
    for (int kk = 0; kk < 20; ++kk) {
        u32x4 g0, g1;
#pragma unroll
        for (int mt = 0; mt < 6; ++mt) {
            acc[mt][0] = mfma(A[kk & 1][mt], f0, acc[mt][0]);
            acc[mt][1] = mfma(A[kk & 1][mt], f1, acc[mt][1]);
            if (kk < 19) A[(kk + 1) & 1][mt] = WL[(mt * 20 + kk + 1) * 64];
            if (mt == 0) gr0 = mfma(f0, f0, gr0);
            if (mt == 1) gr1 = mfma(f1, f1, gr1);
            if (mt == 2 && kk < 19) {
                const int s = (kk + 1) % DP;
                g0 = (u32x4){l[s][0][0], l[s][0][1], l[s][1][0], l[s][1][1]};
                g1 = (u32x4){l[s][0][2], l[s][0][3], l[s][1][2], l[s][1][3]};
            }
            if ((mt == 0 || mt == 3) && kk + DP < 20) l[kk % DP][mt ? 1 : 0] = cld16(rws, xo + (8 * (kk + DP) + 4 * (mt ? 1 : 0)) * GS);
            if (L && mt == 5) xg[kk / 10][(kk % 10) >> 1][kk & 1] = cld16(rws, go + (8 * ((kk % 10) >> 1) + 4 * (kk & 1)) * GS + (((kk / 10) ^ (w >> 1)) * 256u));
            SB();
        }
        if (kk < 19) { f0 = g0; f1 = g1; }
        }

    float rs[2];
    {
        const int r = n & 3;
        const float s0 = r == 0 ? gr0[0] : r == 1 ? gr0[1] : r == 2 ? gr0[2] : gr0[3];
        const float s1 = r == 0 ? gr1[0] : r == 1 ? gr1[1] : r == 2 ? gr1[2] : gr1[3];
        const bool dg = q == (n >> 2);
        rs[0] = __builtin_amdgcn_rsqf(__builtin_fmaf(sumq(dg ? s0 : 0.f), 1.f / 640.f, 1e-5f));
        rs[1] = __builtin_amdgcn_rsqf(__builtin_fmaf(sumq(dg ? s1 : 0.f), 1.f / 640.f, 1e-5f));
    }
    constexpr float C0 = -2.3022081985f, C1 = -0.1029432397f;
#pragma unroll
    for (int hf = 0; hf < 2; ++hf)
#pragma unroll
        for (int u = 0; u < 3; ++u) {
            u32x4 o;
#pragma unroll
            for (int e = 0; e < 2; ++e) {
                const int mt = 2 * u + e, gi = (hf * 3 + u) * 2 + e;
#pragma unroll
                for (int i = 3 * gi; i < 3 * gi + 3 && i < 35; ++i) W[i % 5][i / 5] = cld16(rws, wo + ((i % 5) * 48 + i / 5) * 1024u);
                float hv[4];
#pragma unroll
                for (int r = 0; r < 4; ++r) {
                    const float bv = (r & 1) ? bhi(bb[mt][r >> 1]) : blo(bb[mt][r >> 1]);
                    const float x = __builtin_fmaf(acc[mt][hf][r], rs[hf], bv);
                    const float ex = __builtin_amdgcn_exp2f(x * __builtin_fmaf(x * x, C1, C0));
                    hv[r] = x * __builtin_amdgcn_rcpf(1.f + ex);
                }
                o[2 * e] = pk(hv[0], hv[1]); o[2 * e + 1] = pk(hv[2], hv[3]);
                SB();
            }
            cst16(rws, o_hf + (((2 * blk + hf) * 48 + 3 * fs + u) * 64 + lane) * 16u, o);
        }

    publish(p.ws, L ? PH_G1B : PH_G1A, tt * 16 + fs, ep);

}

#define HSTREAM(o_hf, ph, stamp) \
    wait_flags(p.ws, ph, (rb >> 1) * 16 + 4 * w + (lane & 3), ep); \
 \
    u32x4 hb[3][4]; \
    const unsigned ho = o_hf + ((4 * rb * 48 + 12 * w) * 64 + lane) * 16u; \
    _Pragma("unroll") for (int u = 0; u < 3; ++u) { _Pragma("unroll") for (int j = 0; j < 4; ++j) hb[u][j] = cld16(rws, ho + ((j ^ w) * 48 + u) * 1024u); } \
    SB()

DI void phase_g2(const Params& p, int rb, int ct, u32x4 (&W)[5][12], unsigned wo, u64 ep, char* scr) {
    IDS;
    const rsrc_t rws = mkrs(p.ws, WS_USED);
#pragma unroll
    for (int i = 35; i < 60; ++i) W[i % 5][i / 5] = cld16(rws, wo + ((i % 5) * 48 + i / 5) * 1024u);
    SB();
    HSTREAM(O_HF1, PH_G1A, 21);
    f32x4 acc[4][5];
    const f32x4 z = {0.f, 0.f, 0.f, 0.f};
    u32x2 res[5]; f32x4 bv[5];
#pragma unroll
    for (int u = 0; u < 10; ++u) {
#pragma unroll
        for (int j = 0; j < 4; ++j) {
#pragma unroll
            for (int mt = 0; mt < 5; ++mt) acc[j][mt] = mfma(W[mt][u], hb[u % 3][j], u == 0 ? z : acc[j][mt]);
            if (u + 3 < 12) hb[u % 3][j] = cld16(rws, ho + ((j ^ w) * 48 + u + 3) * 1024u);
            SB();
        }
        if (u == 8) {
#pragma unroll
            for (int mt = 0; mt < 5; ++mt) {
                res[mt] = cld8(rws, O_XH1 + (20 * ct + 4 * mt + q) * GS + (2 * rb + (w >> 1)) * 256u + n * 16u + (w & 1) * 8u);
                bv[mt] = __builtin_bit_cast(f32x4, cld16(rws, O_B2P + (80 * ct + 16 * mt + 4 * q) * 4u));
            }
            SB();
        }
        }
    ktail<5, 640>(acc, (f32x4*)scr, w, lane, [&](int j, int part) {
#pragma unroll
        for (int mt = 0; mt < 5; ++mt) acc[j][mt] = mfma(W[mt][10 + part], hb[(10 + part) % 3][j], acc[j][mt]);
    });

#pragma unroll
    for (int mt = 0; mt < 5; ++mt) {
        acc[0][mt] += bv[mt];
        acc[0][mt][0] += blo(res[mt][0]); acc[0][mt][1] += bhi(res[mt][0]); acc[0][mt][2] += blo(res[mt][1]); acc[0][mt][3] += bhi(res[mt][1]);
    }
    store_xh(acc[0], rws, O_XH2, O_ST2, rb, ct, w, lane, scr);

    publish(p.ws, PH_G2, rb * 8 + ct, ep);

}

DI void phase_g4(const Params& p, int rb, int ct, u32x4 (&W)[5][12], unsigned wo, const u32x4 (&QT)[5][5], const u32x4 (&xg)[2][5][2], u64 ep, char* scr) {
    IDS;
    const rsrc_t rws = mkrs(p.ws, WS_USED), rb2 = mkrs(p.b2, D * 2), rout = mkrs(p.out, S * D * 2);
    f32x4 acc[4][5];
    const f32x4 z = {0.f, 0.f, 0.f, 0.f};
    const bool odd = w & 1;
#pragma unroll
    for (int b = 0; b < 2; ++b)
#pragma unroll
        for (int kk = 0; kk < 5; ++kk) {
            const u32x4 x0 = xg[b][kk][0], x1 = xg[b][kk][1];
            const u32x4 lo = {x0[0], x0[1], x1[0], x1[1]}, hi = {x0[2], x0[3], x1[2], x1[3]};
            const u32x4 fa = odd ? hi : lo, fb = odd ? lo : hi;
#pragma unroll
            for (int mt = 0; mt < 5; ++mt) acc[2 * b][mt] = mfma(QT[mt][kk], fa, kk == 0 ? z : acc[2 * b][mt]);
#pragma unroll
            for (int mt = 0; mt < 5; ++mt) acc[2 * b + 1][mt] = mfma(QT[mt][kk], fb, kk == 0 ? z : acc[2 * b + 1][mt]);
            const int s = b * 5 + kk;
#pragma unroll
            for (int i = 35 + (5 * s) / 2; i < 35 + (5 * (s + 1)) / 2; ++i) W[i % 5][i / 5] = cld16(rws, wo + ((i % 5) * 48 + i / 5) * 1024u);
            SB();
        }

    HSTREAM(O_HF2, PH_G1B, 41);
    u32x2 bb[5];
#pragma unroll
    for (int u = 0; u < 10; ++u) {
#pragma unroll
        for (int j = 0; j < 4; ++j) {
#pragma unroll
            for (int mt = 0; mt < 5; ++mt) acc[j][mt] = mfma(W[mt][u], hb[u % 3][j], acc[j][mt]);
            if (u + 3 < 12) hb[u % 3][j] = cld16(rws, ho + ((j ^ w) * 48 + u + 3) * 1024u);
            SB();
        }
        if (u == 8) {
#pragma unroll
            for (int mt = 0; mt < 5; ++mt) bb[mt] = ld8(rb2, (80 * ct + 16 * mt + 4 * q) * 2u);
            SB();
        }
        }
    ktail<5, 640>(acc, (f32x4*)scr, w, lane, [&](int j, int part) {
#pragma unroll
        for (int mt = 0; mt < 5; ++mt) acc[j][mt] = mfma(W[mt][10 + part], hb[(10 + part) % 3][j], acc[j][mt]);
    });

#pragma unroll
    for (int mt = 0; mt < 5; ++mt) {
        const unsigned d0 = pk(acc[0][mt][0] + blo(bb[mt][0]), acc[0][mt][1] + bhi(bb[mt][0]));
        const unsigned d1 = pk(acc[0][mt][2] + blo(bb[mt][1]), acc[0][mt][3] + bhi(bb[mt][1]));
        st8(rout, (64 * rb + 16 * w + n) * 1280u + (80 * ct + 16 * mt + 4 * q) * 2u, (u32x2){d0, d1});
    }

}

__global__ void __launch_bounds__(256, 1) k_all(Params p) {
    __shared__ __attribute__((aligned(16))) char smem[163840];
    char* scr = smem + LDS_SCR;

    unsigned pf_code;
    {
        u64 pc; unsigned len;
        asm volatile("s_getpc_b64 %0\nkall_pf_begin:\n\ts_mov_b32 %1, kall_pf_end-kall_pf_begin" : "=s"(pc), "=s"(len));
        pf_code = ld4(mkrs((const void*)pc, len), (blockIdx.x >> 3) * 4096u + threadIdx.x * 16u);
    }
#define MAP const int id = fresh_s(blockIdx.x); const Map mp = get_map(id)
    GAS u64* epw = (GAS u64*)(p.ws + O_EPO);
    const u64 ep_in = __hip_atomic_load(epw, __ATOMIC_RELAXED, __HIP_MEMORY_SCOPE_AGENT);

    u64 ep;
    {
        MAP;

        if (mp.isC) {
            phase_p0(p, mp.g, smem);
            ep = rfl64(ep_in) + 1;
            publish(p.ws, PH_P0, mp.g, ep);

            phase_c<0>(p, mp.g, mp.m, ep, smem, scr);
        } else {
            phase_aux(p, mp.ar, mp.m, ep_in, ep, smem);
        }
        PIN(pf_code);
    }

    u32x4 W[5][12], QT[5][5], xg[2][5][2];
    {
        MAP; IDS;
        const unsigned wo = O_W2F + ((5 * mp.ct * 48 + 12 * w) * 64 + lane) * 16u;
        phase_g1<0>(p, mp.tt, mp.m, mp.rb, mp.ct, W, wo, xg, ep, smem);
        phase_g2(p, mp.rb, mp.ct, W, wo, ep, scr);
    }

    {
        MAP;
        if (mp.isC) phase_c<1>(p, mp.g, mp.m, ep, smem, scr);
        else {
            phase_a3(p, mp.ar, ep, scr);
            const int lane = fresh_tid() & 63;
            wait_flags(p.ws, PH_A3, a3_idx(lane), ep);
            wait_flags(p.ws, PH_A3, a3_idx2(lane), ep);
        }
    }
    {
        MAP; IDS;
        const rsrc_t rws = mkrs(p.ws, WS_USED);
#pragma unroll
        for (int kk = 0; kk < 5; ++kk)
#pragma unroll
            for (int mt = 0; mt < 5; ++mt) QT[mt][kk] = cld16(rws, O_QT + (((5 * mp.ct + mt) * 20 + 5 * w + kk) * 64 + lane) * 16u);
        SB();

        const unsigned wo = O_W2T + ((5 * mp.ct * 48 + 12 * w) * 64 + lane) * 16u;
        phase_g1<1>(p, mp.tt, mp.m, mp.rb, mp.ct, W, wo, xg, ep, smem);
        phase_g4(p, mp.rb, mp.ct, W, wo, QT, xg, ep, scr);
    }
    if (blockIdx.x == 0 && threadIdx.x == 0) __hip_atomic_store(epw, ep, __ATOMIC_RELAXED, __HIP_MEMORY_SCOPE_AGENT);

    asm volatile("kall_pf_end:");
}

extern "C" void kernel_launch(void* const* d_in, const int* in_sizes, int n_in, void* d_out, int out_size, void* d_ws, size_t ws_size,
                              hipStream_t stream) {
    Params p;
    p.x = (const uint16_t*)d_in[0]; p.wr = (const uint16_t*)d_in[1]; p.wi = (const uint16_t*)d_in[2];
    p.w1 = (const uint16_t*)d_in[4]; p.b1 = (const uint16_t*)d_in[5]; p.w2 = (const uint16_t*)d_in[6]; p.b2 = (const uint16_t*)d_in[7];
    p.out = (uint16_t*)d_out; p.ws = (char*)d_ws;
    k_all<<<256, 256, 0, stream>>>(p);
}
```

```cpp
#include <hip/hip_runtime.h>
#include <stdint.h>

#pragma clang diagnostic ignored "-Winline-asm"
typedef float f32x4 __attribute__((ext_vector_type(4)));
typedef float f32x2 __attribute__((ext_vector_type(2)));
typedef unsigned u32x4 __attribute__((ext_vector_type(4)));
typedef unsigned u32x2 __attribute__((ext_vector_type(2)));
typedef __bf16 bf16x8 __attribute__((ext_vector_type(8)));
typedef __bf16 bf16x2 __attribute__((ext_vector_type(2)));
typedef __amdgpu_buffer_rsrc_t rsrc_t;
typedef unsigned long long u64;
#define DI __device__ __forceinline__
#define GAS __attribute__((address_space(1)))
#define LAS __attribute__((address_space(3)))

constexpr int S = 2048, D = 640, F = 1536;
#define GSPAD 0
constexpr unsigned GS = 16384u + GSPAD;
constexpr unsigned XH_BYTES = 160u * GS;
constexpr unsigned O_XH0 = 0, O_XH1 = XH_BYTES, O_XH2 = 2 * XH_BYTES, O_XH3 = 3 * XH_BYTES;
constexpr unsigned O_HF1 = 0x0B00000, O_HF2 = 0x1100000;
constexpr unsigned O_W1F = 0x1700000;
constexpr unsigned O_W2F = 0x1900000;
constexpr unsigned O_W2T = 0x1B00000;
constexpr unsigned O_WFQ = 0x1D00000;
constexpr unsigned O_ST0 = 0x1F80000, O_ST2 = 0x1F90000;
constexpr unsigned O_FLG = 0x1FA0000;
constexpr unsigned O_EPO = 0x1FF0000;
constexpr unsigned O_QT = 0x2000000;
constexpr unsigned O_B2P = 0x20D0000;
constexpr unsigned WS_USED = 0x2100000;
constexpr unsigned LDS_SCR = 122880;
enum { PH_P0 = 0, PH_A1, PH_A2, PH_A3, PH_C1, PH_G1A, PH_G2, PH_C2, PH_G1B };

struct Params {
    const uint16_t* x; const uint16_t* wr; const uint16_t* wi; const uint16_t* w1; const uint16_t* b1;
    const uint16_t* w2; const uint16_t* b2; uint16_t* out; char* ws;
};

#define PIN(x) asm volatile("" : "+v"(x))

DI rsrc_t mkrs(const void* p, unsigned bytes) { return __builtin_amdgcn_make_buffer_rsrc((void*)p, 0, bytes, 0x00020000); }
DI u32x4 ld16(rsrc_t r, unsigned off) { return __builtin_amdgcn_raw_buffer_load_b128(r, off, 0, 0); }
DI u32x2 ld8(rsrc_t r, unsigned off) { return __builtin_amdgcn_raw_buffer_load_b64(r, off, 0, 0); }
DI unsigned ld4(rsrc_t r, unsigned off) { return __builtin_amdgcn_raw_buffer_load_b32(r, off, 0, 0); }
DI unsigned ld2(rsrc_t r, unsigned off) { return __builtin_amdgcn_raw_buffer_load_b16(r, off, 0, 0); }
DI void st16(rsrc_t r, unsigned off, u32x4 v) { __builtin_amdgcn_raw_buffer_store_b128(v, r, off, 0, 0); }
DI void st8(rsrc_t r, unsigned off, u32x2 v) { __builtin_amdgcn_raw_buffer_store_b64(v, r, off, 0, 0); }
DI u32x4 cld16(rsrc_t r, unsigned off) { return __builtin_amdgcn_raw_buffer_load_b128(r, off, 0, 16); }
DI u32x2 cld8(rsrc_t r, unsigned off) { return __builtin_amdgcn_raw_buffer_load_b64(r, off, 0, 16); }
DI void cst16(rsrc_t r, unsigned off, u32x4 v) { __builtin_amdgcn_raw_buffer_store_b128(v, r, off, 0, 16); }
DI void cst8(rsrc_t r, unsigned off, u32x2 v) { __builtin_amdgcn_raw_buffer_store_b64(v, r, off, 0, 16); }
DI void cst4(rsrc_t r, unsigned off, unsigned v) { __builtin_amdgcn_raw_buffer_store_b32(v, r, off, 0, 16); }
DI void dma16(rsrc_t r, unsigned voff, unsigned soff, unsigned lds) {
    asm volatile("s_mov_b32 m0, %0\n\ts_nop 0\n\tbuffer_load_dwordx4 %1, %2, %3 offen lds" :: "s"(lds), "v"(voff), "s"(r), "s"(soff) : "m0", "memory");
}
DI void cdma16(rsrc_t r, unsigned voff, unsigned soff, unsigned lds) {
    asm volatile("s_mov_b32 m0, %0\n\ts_nop 0\n\tbuffer_load_dwordx4 %1, %2, %3 offen sc1 lds" :: "s"(lds), "v"(voff), "s"(r), "s"(soff) : "m0", "memory");
}
#define VMWAIT(n) asm volatile("s_waitcnt vmcnt(" #n ")" ::: "memory")
#define VMBAR(n) asm volatile("s_waitcnt vmcnt(" #n ")\n\ts_barrier" ::: "memory")
#define VMLBAR(n) asm volatile("s_waitcnt vmcnt(" #n ") lgkmcnt(0)\n\ts_barrier" ::: "memory")
typedef short s16x4 __attribute__((ext_vector_type(4)));
DI u32x2 trd(const char* a) { return __builtin_bit_cast(u32x2, __builtin_amdgcn_ds_read_tr16_b64_v4i16((LAS s16x4*)a)); }
DI f32x4 mfma(u32x4 a, u32x4 b, f32x4 c) {
    return __builtin_amdgcn_mfma_f32_16x16x32_bf16(__builtin_bit_cast(bf16x8, a), __builtin_bit_cast(bf16x8, b), c, 0, 0, 0);
}
DI unsigned pk(float lo, float hi) { bf16x2 v = __builtin_convertvector((f32x2){lo, hi}, bf16x2); return __builtin_bit_cast(unsigned, v); }
DI float blo(unsigned u) { return __uint_as_float(u << 16); }
DI float bhi(unsigned u) { return __uint_as_float(u & 0xffff0000u); }
DI float dot2(unsigned a, unsigned b, float c) { return __builtin_amdgcn_fdot2_f32_bf16(__builtin_bit_cast(bf16x2, a), __builtin_bit_cast(bf16x2, b), c, false); }
DI float vcos(float r) { return __builtin_amdgcn_cosf(r); }
DI float vsin(float r) { return __builtin_amdgcn_sinf(r); }
DI float fract(float r) { return __builtin_amdgcn_fractf(r); }
DI void bar() { asm volatile("s_waitcnt lgkmcnt(0)\n\ts_barrier" ::: "memory"); }
DI float sumq(float v) {
    auto a = __builtin_amdgcn_permlane16_swap(__float_as_uint(v), __float_as_uint(v), false, false);
    v = __uint_as_float(a[0]) + __uint_as_float(a[1]);
    auto b = __builtin_amdgcn_permlane32_swap(__float_as_uint(v), __float_as_uint(v), false, false);
    return __uint_as_float(b[0]) + __uint_as_float(b[1]);
}
#define SB() __builtin_amdgcn_sched_barrier(0)
DI int fresh_tid() { int t = threadIdx.x; asm volatile("" : "+v"(t)); return t; }
DI unsigned rfl(unsigned v) { return (unsigned)__builtin_amdgcn_readfirstlane((int)v); }
DI u64 rfl64(u64 v) { return (u64)rfl((unsigned)(v >> 32)) << 32 | (u64)rfl((unsigned)v); }
DI int fresh_s(int v) { asm volatile("" : "+s"(v)); return v; }
#define IDS const int tid = fresh_tid(), w = __builtin_amdgcn_readfirstlane(tid >> 6), lane = tid & 63, n = lane & 15, q = lane >> 4

DI GAS u64* flag_ptr(char* ws, int e) { return (GAS u64*)(ws + O_FLG + e * 128); }
DI void publish(char* ws, int ph, int idx, u64 ep) {
    asm volatile("s_waitcnt vmcnt(0)" ::: "memory");
    bar();
    if (threadIdx.x == 0) __hip_atomic_store(flag_ptr(ws, ph * 256 + idx), ep, __ATOMIC_RELAXED, __HIP_MEMORY_SCOPE_AGENT);
}
DI void wait_flags(char* ws, int ph, int idx, u64 ep) {
    GAS u64* f = flag_ptr(ws, ph * 256 + idx);
    for (int s = 0; s < 400000; ++s) {
        const u64 v = __hip_atomic_load(f, __ATOMIC_RELAXED, __HIP_MEMORY_SCOPE_AGENT);
        if (__all(v == ep)) break;
        __builtin_amdgcn_s_sleep(1);
    }
    asm volatile("" ::: "memory");
}

DI u64 peek_flag(char* ws, int ph, int idx) {
    return __hip_atomic_load(flag_ptr(ws, ph * 256 + idx), __ATOMIC_RELAXED, __HIP_MEMORY_SCOPE_AGENT);
}
DI void finish_wait(char* ws, int ph, int idx, u64 ep, u64 first) {
    if (!__all(first == ep)) wait_flags(ws, ph, idx, ep);
    asm volatile("" ::: "memory");
}

constexpr float AMP = 0.05590169943749474f;
constexpr float AMP0 = 0.03952847075210474f;

struct Map { int tt, m, rb, ct, isC, g, ar; };
__host__ __device__ inline Map get_map(int id) {
    Map r;
    const int xcd = id & 7, slot = id >> 3;
    r.tt = xcd + 8 * (slot >> 4); r.m = slot & 15; r.rb = 2 * r.tt + (r.m >> 3); r.ct = r.m & 7;
    const bool aux = slot >= 24 || (slot >= 16 && ((slot >> 2) & 1) == (xcd & 1));
    r.isC = !aux;
    r.g = 20 * xcd + (slot < 16 ? slot : 16 + (slot & 3));
    r.ar = 12 * xcd + (slot >= 24 ? slot - 24 : 8 + (slot & 3));
    return r;
}

template <class H>
DI void gen_q(u32x4 (&Q)[2][20], int cg, int w, int lane, H hook) {
    const int n = lane & 15, q = lane >> 4;
    f32x2 dl, r, d32, c2;
#pragma unroll
    for (int mt = 0; mt < 2; ++mt) {
        const int c = 128 * cg + 32 * w + 16 * mt + n;
        const float pf = c == 1 ? 320.f : (float)(c >> 1);
        const float ph = ((c & 1) && c != 1) ? 0.25f : 0.f;
        dl[mt] = pf * (1.f / 640.f);
        const float t = pf * (float)(128 * cg + 8 * q);
        const float k = floorf(t * (1.f / 640.f));
        r[mt] = __builtin_fmaf(-k, 640.f, t) * (1.f / 640.f) + ph;
        d32[mt] = fract(pf * 0.05f);
        c2[mt] = 2.f * vcos(dl[mt]);
    }
#pragma unroll
    for (int kk = 0; kk < 20; ++kk) {
        hook(kk);
        const f32x2 r1 = r + dl;
        f32x2 v[8];
        v[0] = (f32x2){vcos(r[0]), vcos(r[1])};
        v[1] = (f32x2){vcos(r1[0]), vcos(r1[1])};
#pragma unroll
        for (int j = 2; j < 8; ++j) v[j] = __builtin_elementwise_fma(c2, v[j - 1], -v[j - 2]);
#pragma unroll
        for (int mt = 0; mt < 2; ++mt)
            Q[mt][kk] = (u32x4){pk(v[0][mt], v[1][mt]), pk(v[2][mt], v[3][mt]), pk(v[4][mt], v[5][mt]), pk(v[6][mt], v[7][mt])};
        r = r + d32;
        r = (f32x2){fract(r[0]), fract(r[1])};
        PIN(Q[0][kk]); PIN(Q[1][kk]);
        SB();
    }
}

template <int NA, int WS, class MM>
DI void ktail(f32x4 (&acc)[4][NA], f32x4* sb, int w, int lane, MM mm) {
    f32x4* wr = sb + w * WS + lane;
    f32x4* rd = sb + (w ^ 2) * WS + lane;
    const f32x4* rd2 = sb + (w ^ 3) * WS + NA * 64 + lane;
    mm(2, 0); mm(2, 1); mm(3, 0); mm(3, 1);
    SB();
    mm(1, 0);
#pragma unroll
    for (int m = 0; m < NA; ++m) wr[m * 64] = acc[2][m];
    SB();
    mm(1, 1);
#pragma unroll
    for (int m = 0; m < NA; ++m) wr[(NA + m) * 64] = acc[3][m];
    SB();
    bar();
    f32x4 in1[NA], in0[NA];
#pragma unroll
    for (int m = 0; m < NA; ++m) in1[m] = rd[(NA + m) * 64];
    SB();
    mm(0, 0);
    SB();
#pragma unroll
    for (int m = 0; m < NA; ++m) rd[(NA + m) * 64] = acc[1][m] + in1[m];
    SB();
#pragma unroll
    for (int m = 0; m < NA; ++m) in0[m] = rd[m * 64];
    mm(0, 1);
    SB();
    bar();
#pragma unroll
    for (int m = 0; m < NA; ++m) in1[m] = rd2[m * 64];
#pragma unroll
    for (int m = 0; m < NA; ++m) acc[0][m] += in0[m];
    SB();
#pragma unroll
    for (int m = 0; m < NA; ++m) acc[0][m] += in1[m];
}

DI void store_xh(const f32x4 (&v)[5], rsrc_t rws, unsigned o_xh, unsigned o_st, int rb, int ct, int t, int lane, char* sb) {
    const int n = lane & 15, q = lane >> 4;
    u32x2 d[5];
    float ss = 0.f;
#pragma unroll
    for (int mt = 0; mt < 5; ++mt) {
        d[mt] = (u32x2){pk(v[mt][0], v[mt][1]), pk(v[mt][2], v[mt][3])};
        const unsigned m0 = (mt == 0 && ct == 0 && q == 0) ? (d[mt][0] & 0xffff0000u) : d[mt][0];
        ss = dot2(m0, m0, ss); ss = dot2(d[mt][1], d[mt][1], ss);
    }
    u32x2* mine = (u32x2*)(sb + (t ^ 2) * 10240) + lane;
    const u32x2* other = (const u32x2*)(sb + (t ^ 3) * 10240) + lane;
    const unsigned base = o_xh + (20 * ct + q) * GS + (2 * rb + (t >> 1)) * 256u + n * 16u;
    if (t & 1) {
        mine[0] = d[0]; mine[64] = d[1]; mine[128] = d[2];
        ss = sumq(ss);
        mine[192] = (u32x2){__float_as_uint(ss), 0u};
        bar();
        const u32x2 a = other[0], b = other[64];
        cst16(rws, base + 12 * GS, (u32x4){a[0], a[1], d[3][0], d[3][1]});
        cst16(rws, base + 16 * GS, (u32x4){b[0], b[1], d[4][0], d[4][1]});
    } else {
        mine[0] = d[3]; mine[64] = d[4];
        ss = sumq(ss);
        bar();
        const u32x2 a = other[0], b = other[64], c = other[128], s1 = other[192];
        cst16(rws, base, (u32x4){d[0][0], d[0][1], a[0], a[1]});
        cst16(rws, base + 4 * GS, (u32x4){d[1][0], d[1][1], b[0], b[1]});
        cst16(rws, base + 8 * GS, (u32x4){d[2][0], d[2][1], c[0], c[1]});
        const unsigned ln = ((t >> 1) + 2 * ((rb >> 2) & 1)) * 16 + n;
        const unsigned so = o_st + ((ct * 4 + (rb >> 3)) * 64 + ln) * 16u + (rb & 3) * 4u;
        cst4(rws, q == 0 ? so : 0xfffffff0u, pk(ss, __uint_as_float(s1[0])));
    }
}

DI int w1f_idx(int m, int lane) { return 5 * m + lane % 5; }
DI void w1f_dma(rsrc_t rws, unsigned lb, int m, int w, int lane, int i) {
    cdma16(rws, lane * 16u, O_W1F + m * 122880u + w * 30720u + i * 1024u, lb + w * 30720u + i * 1024u);
}

DI unsigned rot5(int a, int b) { const int s = a + b; return s >= 5 ? s - 5 : s; }

DI void phase_p0(const Params& p, int g, char* smem) {
    IDS;
    const int rb = g / 5, cg = g - 5 * rb;
    const rsrc_t rws = mkrs(p.ws, WS_USED), rx = mkrs(p.x, S * D * 2);
    const unsigned lb = (unsigned)(size_t)(LAS char*)smem;
    const unsigned sw = q * 1280u + ((n ^ q) << 4);
    const rsrc_t rwr = mkrs(p.wr, S * 321 * 2), rwi = mkrs(p.wi, S * 321 * 2);
    const int ks0 = 16 * (rb >> 4) + 32 * (16 * ((rb >> 2) & 3) + 4 * (rb & 3));
    unsigned fr[2][4], fi[2][4], ny[4];
    const unsigned o1 = ((ks0 + 4 * w + q) * 321 + 64 * cg + 2 * n) * 2u;
    const bool nyq = cg == 0 && w == 1;
    const int ks = ks0 + (lane >> 2) + 32 * (lane & 3), kn = (2048 - ks) & 2047;
    const unsigned oa = nyq ? (ks * 321 + 320) * 2u : 0x80000000u, ob = nyq ? (kn * 321 + 320) * 2u : 0x80000000u;
    u32x4 Q[2][20];
    gen_q(Q, cg, w, lane, [&](int kk) {
        if (kk < 10) {
#pragma unroll
            for (int i = 2 * kk; i < 2 * kk + 2; ++i)
                dma16(rx, sw ^ ((i & 3) << 6), (64 * rb + 16 * w + 4 * (i & 3)) * 1280u + rot5(cg, i >> 2) * 256u, lb + (i >> 2) * 16384u + w * 4096u + (i & 3) * 1024u);
        } else if (kk < 18) {
            const int it = (kk - 10) >> 2, jj = (kk - 10) & 3;
            fr[it][jj] = __builtin_amdgcn_raw_buffer_load_b32(rwr, o1 + it * 64, jj * 32 * 321 * 2, 0);
            fi[it][jj] = __builtin_amdgcn_raw_buffer_load_b32(rwi, o1 + it * 64, jj * 32 * 321 * 2, 0);
        } else if (kk == 18) {
            ny[0] = ld2(rwr, oa); ny[1] = ld2(rwr, ob);
        } else {
            ny[2] = ld2(rwi, oa); ny[3] = ld2(rwi, ob);
        }
    });

    const f32x4 z = {0.f, 0.f, 0.f, 0.f};
    f32x4 acc[2][4];
    f32x4 gr = z, sr = z;
    unsigned one = 0x3f803f80u;
    PIN(one);
    const u32x4 ones = {one, one, one, one};
    const unsigned rb0 = n * 256u + ((n ^ q) << 4);
    auto rd = [&](int kk, int j) -> u32x4 { return *(const u32x4*)(smem + (rb0 ^ ((kk & 3) << 6)) + (kk >> 2) * 16384 + j * 4096); };
    auto rdo = [&](int kk) -> u32x4 { return *(const u32x4*)(smem + ((rb0 ^ ((kk & 3) << 6)) + w * 4096u) + (kk >> 2) * 16384); };
    VMBAR(36);

    u32x4 xa[2][4], xo[2];
#pragma unroll
    for (int j = 0; j < 4; ++j) xa[0][j] = rd(0, j);
    xo[0] = rdo(0);
    SB();
#pragma unroll
    for (int kk = 0; kk < 20; ++kk) {
        if (kk == 3) { VMBAR(32); }
        if (kk == 7) { VMBAR(28); }
        if (kk == 11) {

#pragma unroll
            for (int i = 0; i < 4; ++i) { PIN(ny[i]); PIN(fr[0][i]); PIN(fr[1][i]); PIN(fi[0][i]); PIN(fi[1][i]); }
            const unsigned fo = O_WFQ + ((64 * cg + 2 * n) * 512 + 16 * rb + 4 * w + q) * 16u;
#pragma unroll
            for (int it = 0; it < 2; ++it) {
                u32x4 o0, o1;
#pragma unroll
                for (int jj = 0; jj < 4; ++jj) {
                    o0[jj] = __builtin_amdgcn_perm(fi[it][jj], fr[it][jj], 0x05040100u);
                    o1[jj] = __builtin_amdgcn_perm(fi[it][jj], fr[it][jj], 0x07060302u);
                }
                cst16(rws, (it == 0 && cg == 0 && n == 0) ? 0xfffffff0u : fo + it * 32 * 8192u, o0);
                cst16(rws, fo + it * 32 * 8192u + 8192u, o1);
            }
            cst4(rws, nyq ? O_WFQ + (16 * rb + (lane >> 2)) * 16u + (lane & 3) * 4u : 0xfffffff0u,
                 pk((blo(ny[0]) + blo(ny[1])) * 0.5f, (blo(ny[2]) - blo(ny[3])) * 0.5f));
            SB();

            asm volatile("s_barrier" ::: "memory");

        }
        SB();
#pragma unroll
        for (int j = 0; j < 4; ++j) {
            acc[0][j] = mfma(Q[0][kk], xa[kk & 1][j], kk == 0 ? z : acc[0][j]);
            if (kk < 19) xa[(kk + 1) & 1][j] = rd(kk + 1, j);
            if (j == 1) gr = mfma(xo[kk & 1], xo[kk & 1], gr);
            if (j == 3) sr = mfma(ones, xo[kk & 1], sr);
            acc[1][j] = mfma(Q[1][kk], xa[kk & 1][j], kk == 0 ? z : acc[1][j]);
            if (kk < 19 && j == 0) xo[(kk + 1) & 1] = rdo(kk + 1);
            SB();
        }
    }

    const bool dc = cg == 0 && w == 0 && q == 0;
#pragma unroll
    for (int mt = 0; mt < 2; ++mt)
#pragma unroll
        for (int b = 0; b < 2; ++b) {
            f32x4 v0 = acc[mt][2 * b] * AMP, v1 = acc[mt][2 * b + 1] * AMP;
            if (mt == 0 && dc) { v0[0] *= AMP0 / AMP; v0[1] *= AMP0 / AMP; v1[0] *= AMP0 / AMP; v1[1] *= AMP0 / AMP; }
            cst16(rws, O_XH0 + (32 * cg + 8 * w + 4 * mt + q) * GS + (2 * rb + b) * 256u + n * 16u,
                  (u32x4){pk(v0[0], v0[1]), pk(v0[2], v0[3]), pk(v1[0], v1[1]), pk(v1[2], v1[3])});
        }
    {
        const int r = n & 3;
        const float dg = r == 0 ? gr[0] : r == 1 ? gr[1] : r == 2 ? gr[2] : gr[3];
        const float ss = sumq(q == (n >> 2) ? dg : 0.f), sm = sr[0];
        const float var = fmaxf(__builtin_fmaf(-sm * (1.f / 640.f), sm, ss) * (1.f / 640.f), 0.f);
        const unsigned so = O_ST0 + (((rb >> 3) * 64 + (2 * ((rb >> 2) & 1) + (w >> 1)) * 16 + n) * 2 + (w & 1)) * 16u + (rb & 3) * 4u;
        cst4(rws, (q == 0 && cg == 0) ? so : 0xfffffff0u, __float_as_uint(__builtin_amdgcn_rsqf(var + 1e-5f)));
    }

}

DI void phase_aux(const Params& p, int ar, int m, u64 ep_in, u64& ep, char* smem) {
    IDS;
    const int xcd = ar / 12, a = ar - 12 * xcd;
    const bool act = a < 10;
    const int spare = 2 * xcd + a - 10;
    const int fj = 2 * xcd + (a >= 5), cg = act ? (a >= 5 ? a - 5 : a) : spare;
    const rsrc_t rws = mkrs(p.ws, WS_USED);
    const unsigned lb = (unsigned)(size_t)(LAS char*)smem;
    const f32x4 z = {0.f, 0.f, 0.f, 0.f};
    if (act || spare < 5) {
        const rsrc_t rw1 = mkrs(p.w1, act ? D * F * 2 : 0), rw2 = mkrs(p.w2, act ? D * F * 2 : 0);
        unsigned v1[3];
#pragma unroll
        for (int b = 0; b < 3; ++b) {
            const int ix = 64 * b + lane, rl = ix / 12, cp = ix - 12 * rl;
            v1[b] = rl * 3072u + ((cp ^ ((rl >> 3) << 1)) << 4);
        }
        auto dma1 = [&](int i) {
            const int k = i / 6, u = (i % 6) / 3, b = i % 3;
            dma16(rw1, v1[b], (128 * rot5(cg, k) + 16 * (2 * w + u)) * 3072u + fj * 192u, lb + k * 24576u + (2 * w + u) * 3072u + b * 1024u);
        };
        const unsigned sw = q * 1280u + ((n ^ q) << 4);
        auto dma2 = [&](int s, int t) {
            dma16(rw2, sw ^ (((2 * w + t) & 3) << 6), (96 * fj + 24 * w + 4 * t) * 1280u + rot5(cg, s) * 256u, lb + (s ? (s - 1) * 24576u : 122880u) + w * 6144u + t * 1024u);
        };
        u32x4 Q[2][20];
        gen_q(Q, cg, w, lane, [&](int kk) {
            const int i0 = kk < 16 ? 2 * kk : 16 + kk, i1 = kk < 16 ? 2 * kk + 2 : 17 + kk;
#pragma unroll
            for (int i = i0; i < i1; ++i) { if (i < 30) dma1(i); else dma2(0, i - 30); }
        });

        if (act) {
            const int qq = (lane >> 2) & 3, pp = lane & 3;
            const unsigned tb = (8 * q + qq) * 192u + (pp >> 1) * 16u + (pp & 1) * 8u;
            const unsigned tbe = tb + (q & 1) * 32u, tbo = tb - (q & 1) * 32u;
            auto rd1 = [&](int kk, int ft) -> u32x4 {
                const char* ap = smem + ((ft & 1) ? tbo : tbe) + (kk >> 2) * 24576u;
                const u32x2 lo = trd(ap + (kk & 3) * 6144 + ft * 32), hi = trd(ap + (kk & 3) * 6144 + ft * 32 + 768);
                return (u32x4){lo[0], lo[1], hi[0], hi[1]};
            };
            const unsigned rb0 = n * 256u + ((n ^ q) << 4);
            auto rd2 = [&](int kk, int ft) -> u32x4 {
                const int s = kk >> 2;
                return *(const u32x4*)(smem + ((rb0 ^ ((kk & 3) << 6)) + (s ? (s - 1) * 24576u : 122880u)) + ft * 4096);
            };
            f32x4 acc[2][6];
            u32x4 fa[2][6];
            VMBAR(30);

#pragma unroll
            for (int ft = 0; ft < 6; ++ft) fa[0][ft] = rd1(0, ft);
            SB();
#pragma unroll
            for (int kk = 0; kk < 20; ++kk) {
                if ((kk & 3) == 3) { VMLBAR(24); }
                SB();
#pragma unroll
                for (int ft = 0; ft < 6; ++ft) {
                    acc[0][ft] = mfma(Q[0][kk], fa[kk & 1][ft], kk == 0 ? z : acc[0][ft]);
                    fa[(kk + 1) & 1][ft] = kk < 19 ? rd1(kk + 1, ft) : rd2(0, ft);
                    acc[1][ft] = mfma(Q[1][kk], fa[kk & 1][ft], kk == 0 ? z : acc[1][ft]);
                    if ((kk & 3) == 3 && kk < 19) dma2((kk >> 2) + 1, ft);
                    SB();
                }
            }

            {
                const bool dc = cg == 0 && w == 0 && q == 0;
#pragma unroll
                for (int ft = 0; ft < 6; ++ft) {
                    f32x4 a0 = acc[0][ft] * AMP;
                    const f32x4 a1 = acc[1][ft] * AMP;
                    if (dc) { a0[0] = 0.f; a0[1] *= AMP0 / AMP; }
                    cst16(rws, O_W1F + (((6 * fj + ft) * 20 + 4 * cg + w) * 64 + lane) * 16u, (u32x4){pk(a0[0], a0[1]), pk(a0[2], a0[3]), pk(a1[0], a1[1]), pk(a1[2], a1[3])});
                }
            }
            SB();

#pragma unroll
            for (int kk = 0; kk < 20; ++kk) {
                if (kk == 3) VMBAR(24);
                if (kk == 7) { VMWAIT(0); ep = rfl64(ep_in) + 1; publish(p.ws, PH_A1, 5 * fj + cg, ep); }
                SB();
#pragma unroll
                for (int ft = 0; ft < 6; ++ft) {
                    acc[0][ft] = mfma(fa[kk & 1][ft], Q[0][kk], kk == 0 ? z : acc[0][ft]);
                    acc[1][ft] = mfma(fa[kk & 1][ft], Q[1][kk], kk == 0 ? z : acc[1][ft]);
                    if (kk < 19) fa[(kk + 1) & 1][ft] = rd2(kk + 1, ft);
                    SB();
                }
            }

#pragma unroll
            for (int mt = 0; mt < 2; ++mt) {
                const float am = (mt == 0 && cg == 0 && w == 0 && n < 2) ? AMP0 : AMP;
#pragma unroll
                for (int u = 0; u < 3; ++u) {
                    const f32x4 v0 = acc[mt][2 * u] * am, v1_ = acc[mt][2 * u + 1] * am;
                    cst16(rws, O_W2F + (((8 * cg + 2 * w + mt) * 48 + 3 * fj + u) * 64 + lane) * 16u,
                          (u32x4){pk(v0[0], v0[1]), pk(v0[2], v0[3]), pk(v1_[0], v1_[1]), pk(v1_[2], v1_[3])});
                }
            }
            publish(p.ws, PH_A2, 5 * fj + cg, ep);
        } else {
            const rsrc_t rb2 = mkrs(p.b2, D * 2);
            f32x4 a0 = z, a1 = z;
#pragma unroll
            for (int kk = 0; kk < 20; ++kk) {
                const u32x4 bf = ld16(rb2, (128 * rot5(cg, kk >> 2) + 32 * (kk & 3) + 8 * q) * 2u);
                a0 = mfma(bf, Q[0][kk], a0); a1 = mfma(bf, Q[1][kk], a1);
            }
            const float am = (cg == 0 && w == 0 && n < 2) ? AMP0 : AMP;
            cst4(rws, q < 2 ? O_B2P + (128 * cg + 32 * w + 16 * q + n) * 4u : 0xfffffff0u, __float_as_uint(q == 0 ? a0[0] * am : a1[0] * AMP));
            ep = rfl64(ep_in) + 1;
            publish(p.ws, PH_A2, 80 + cg, ep);
        }
    } else ep = rfl64(ep_in) + 1;

    const int u = ar >> 1, half = ar & 1;
    {
        const rsrc_t rw2 = mkrs(p.w2, D * F * 2);
#pragma unroll
        for (int i = 0; i < 5; ++i) {
            const unsigned o = (w * 5 + i) * 1024u + lane * 16u, row = o / 640u;
            dma16(rw2, (32 * u + row) * 1280u + half * 640u + (o - row * 640u), 0u, lb + LDS_SCR + (w * 5 + i) * 1024u);
        }
    }
    wait_flags(p.ws, PH_A1, w1f_idx(m, lane), ep);

#pragma unroll
    for (int i = 0; i < 30; ++i) w1f_dma(rws, lb, m, w, lane, i);
    VMBAR(30);
#pragma unroll
    for (int i = 0; i < 5; ++i) {
        const int tl = 5 * w + i;
        const char* ap = smem + LDS_SCR + (4 * q + ((lane >> 2) & 3)) * 640 + tl * 32 + (lane & 3) * 8;
        const u32x2 lo = trd(ap), hi = trd(ap + 16 * 640);
        cst16(rws, O_W2T + (((20 * half + tl) * 48 + u) * 64 + lane) * 16u, (u32x4){lo[0], lo[1], hi[0], hi[1]});
    }
    publish(p.ws, PH_A3, ar, ep);
}

DI void gen_qt(u32x4 (&QT)[5][5], int ct, int w, int lane) {
    const int n = lane & 15, q = lane >> 4;
#pragma unroll
    for (int mt = 0; mt < 5; ++mt) {
        const float df = (float)(80 * ct + 16 * mt + n);
        const float t = (float)(80 * w + 2 * q) * df;
        const float k = floorf(t * (1.f / 640.f));
        const float r0 = __builtin_fmaf(-k, 640.f, t) * (1.f / 640.f);
        const float d1 = df * (1.f / 640.f), d8 = fract(df * (1.f / 80.f));
        const float c2 = 2.f * vcos(d8);
        f32x2 a[2], b[2];
#pragma unroll
        for (int pr = 0; pr < 2; ++pr) {
            const float ra = pr ? r0 + d1 : r0, rb = ra + d8;
            a[pr] = (f32x2){vcos(ra), vcos(ra + 0.25f)} * AMP;
            b[pr] = (f32x2){vcos(rb), vcos(rb + 0.25f)} * AMP;
        }
#pragma unroll
        for (int s = 0; s < 10; ++s) {
#pragma unroll
            for (int pr = 0; pr < 2; ++pr) {
                f32x2 v;
                if (s == 0) v = a[pr];
                else if (s == 1) v = b[pr];
                else { v = __builtin_elementwise_fma((f32x2){c2, c2}, b[pr], -a[pr]); a[pr] = b[pr]; b[pr] = v; }
                QT[mt][s >> 1][2 * (s & 1) + pr] = pk(v[0], v[1]);
            }
        }
        if (w == 0 && q == 0) QT[mt][0][0] = pk(AMP0, (n & 1) ? -AMP0 : AMP0);
#pragma unroll
        for (int kk = 0; kk < 5; ++kk) PIN(QT[mt][kk]);
        SB();
    }
}

template <int L>
DI void phase_c(const Params& p, int g, int m1, u64 ep, char* lds0, char* smem) {
    constexpr unsigned o_in = L ? O_XH2 : O_XH0, o_st = L ? O_ST2 : O_ST0, o_out = L ? O_XH3 : O_XH1;
    constexpr int SO = L ? 28 : 8;
    IDS;
    const rsrc_t rws = mkrs(p.ws, WS_USED);
    const int c = w >> 1, kh = w & 1;
    u32x4 B1r[2], B1i[2], A3[4][4], A6[2][2];
    f32x2 twc[4][2], tws[4][2];
    u64 fv;
    {
        const float ka = (float)(16 * kh + n);
#pragma unroll
        for (int ks = 0; ks < 2; ++ks)
#pragma unroll
            for (int m = 0; m < 4; ++m) {
                const float r = (float)(16 * ks + 4 * q + m) * (1.f / 32.f) * ka;
                const float cs = vcos(r), sn = vsin(r);
                B1r[ks][m] = pk(cs, sn); B1i[ks][m] = pk(-sn, cs);
            }
#pragma unroll
        for (int bt = 0; bt < 4; ++bt)
#pragma unroll
            for (int r = 0; r < 4; ++r) {
                const float a = (float)(16 * bt + 4 * q + r) * ka * (1.f / 2048.f);
                twc[bt][r >> 1][r & 1] = vcos(a); tws[bt][r >> 1][r & 1] = vsin(a);
            }
        SB();
        fv = peek_flag(p.ws, L ? PH_G2 : PH_P0, L || tid < 160 ? tid : 0);
        SB();
#pragma unroll
        for (int bt = 0; bt < 4; ++bt)
#pragma unroll
            for (int m = 0; m < 4; ++m) {
                const float a = (float)n * (float)(16 * bt + 4 * q + m) * (1.f / 64.f);
                const float cs = vcos(a), sn = vsin(a);
#pragma unroll
                for (int mt = 0; mt < 4; ++mt) {
                    const int k4 = (mt * m) & 3;
                    A3[mt][bt][m] = k4 == 0 ? pk(cs, sn) : k4 == 1 ? pk(-sn, cs) : k4 == 2 ? pk(-cs, -sn) : pk(sn, -cs);
                }
            }
        const float rio = (float)(n & 1) * -0.25f;
#pragma unroll
        for (int rt = 0; rt < 2; ++rt)
#pragma unroll
            for (int ks = 0; ks < 2; ++ks)
#pragma unroll
                for (int m = 0; m < 4; ++m) {
                    const float a = (float)(16 * (w >> 1) + 8 * rt + (n >> 1)) * (float)(16 * ks + 4 * q + m) * (1.f / 32.f) + rio;
                    A6[rt][ks][m] = pk(vcos(a) * (1.f / 2048.f), vcos(a + 0.25f) * (1.f / 2048.f));
                }
#pragma unroll
        for (int i = 0; i < 4; ++i) {
            if (i < 2) { PIN(B1r[i]); PIN(B1i[i]); PIN(A6[i][0]); PIN(A6[i][1]); }
            PIN(A3[i][0]); PIN(A3[i][1]); PIN(A3[i][2]); PIN(A3[i][3]);
            PIN(twc[i][0]); PIN(twc[i][1]); PIN(tws[i][0]); PIN(tws[i][1]);
        }
    }
    SB();

    finish_wait(p.ws, L ? PH_G2 : PH_P0, L || tid < 160 ? tid : 0, ep, fv);
    bar();

    u32x4 xin[4], st[L ? 8 : 2], wf[4];
    {
        const int h = q & 1, hi = q >> 1;
#pragma unroll
        for (int mt = 0; mt < 4; ++mt) wf[mt] = cld16(rws, O_WFQ + ((2 * g + c) * 512 + (kh * 4 + mt) * 64 + lane) * 16u);
        if constexpr (L) {
#pragma unroll
            for (int cp = 0; cp < 8; ++cp) st[cp] = cld16(rws, o_st + ((cp * 4 + w) * 64 + lane) * 16u);
        } else {
#pragma unroll
            for (int hf = 0; hf < 2; ++hf) st[hf] = cld16(rws, o_st + ((w * 64 + lane) * 2 + hf) * 16u);
        }
#pragma unroll
        for (int ii = 0; ii < 4; ++ii) xin[ii] = cld16(rws, o_in + g * GS + (2 * (8 * w + 4 * hi + ii) + h) * 256u + n * 16u);
        if (!L) fv = peek_flag(p.ws, PH_A1, w1f_idx(m1, lane));
    }
    SB();
    u32x4* FR = (u32x4*)smem;
    u32x4* RAW = (u32x4*)(smem + 16384);
    {
        const int h = q & 1, hi = q >> 1;
        float rs[4][2];
        const unsigned sel0 = (unsigned)fresh_s(0x00003f80), sel1 = (unsigned)fresh_s(0x3f800000);
#pragma unroll
        for (int ii = 0; ii < 4; ++ii)
#pragma unroll
            for (int hf = 0; hf < 2; ++hf) {
                if constexpr (L) {
                    float s = 0.f;
#pragma unroll
                    for (int cp = 0; cp < 8; ++cp) s = dot2(st[cp][ii], hf ? sel1 : sel0, s);
                    rs[ii][hf] = __builtin_amdgcn_rsqf(__builtin_fmaf(s, 1.f / 640.f, 1e-5f));
                } else rs[ii][hf] = __uint_as_float(st[hf][ii]);
            }
#pragma unroll
        for (int ii = 0; ii < 4; ++ii) RAW[((8 * w + 4 * hi + ii) * 2 + h) * 16 + n] = xin[ii];
        const int aq = 2 * w + hi;
#pragma unroll
        for (int hf = 0; hf < 2; ++hf)
#pragma unroll
            for (int cc = 0; cc < 2; ++cc) {
                u32x4 o;
#pragma unroll
                for (int ii = 0; ii < 4; ++ii) {
                    const unsigned u = xin[ii][hf * 2 + cc];
                    float re = blo(u) * rs[ii][hf];
                    if (cc == 0 && g == 0) re = 0.f;
                    o[ii] = pk(re, bhi(u) * rs[ii][hf]);
                }
                FR[((cc * 4 + 2 * h + hf) * 2 + (aq >> 2)) * 64 + (aq & 3) * 16 + n] = o;
            }
    }
    bar();

    const unsigned lb = (unsigned)(size_t)(LAS char*)lds0;
    if (!L) finish_wait(p.ws, PH_A1, w1f_idx(m1, lane), ep, fv);
    f32x4 tr[4], ti[4];
    {
        u32x4 a[4][2];
#pragma unroll
        for (int bt = 0; bt < 4; ++bt) { a[bt][0] = FR[((c * 4 + bt) * 2 + 0) * 64 + lane]; a[bt][1] = FR[((c * 4 + bt) * 2 + 1) * 64 + lane]; }
        SB();
#pragma unroll
        for (int bt = 0; bt < 4; ++bt) {
            const f32x4 z = {0.f, 0.f, 0.f, 0.f};
            tr[bt] = mfma(a[bt][1], B1r[1], mfma(a[bt][0], B1r[0], z));
            ti[bt] = mfma(a[bt][1], B1i[1], mfma(a[bt][0], B1i[0], z));
            if (!L) { w1f_dma(rws, lb, m1, w, lane, bt); SB(); }
        }
    }
    bar();
    u32x4 P[4], Pn[4];
#pragma unroll
    for (int bt = 0; bt < 4; ++bt)
#pragma unroll
        for (int r2 = 0; r2 < 2; ++r2) {
            const f32x2 a = {tr[bt][2 * r2], tr[bt][2 * r2 + 1]}, b = {ti[bt][2 * r2], ti[bt][2 * r2 + 1]};
            const f32x2 xr = a * twc[bt][r2] + b * tws[bt][r2], xi = b * twc[bt][r2] - a * tws[bt][r2];
            P[bt][2 * r2] = pk(xr[0], xi[0]); P[bt][2 * r2 + 1] = pk(xr[1], xi[1]);
            Pn[bt][2 * r2] = pk(xi[0], -xr[0]); Pn[bt][2 * r2 + 1] = pk(xi[1], -xr[1]);
            if (!L && r2 == 1) { w1f_dma(rws, lb, m1, w, lane, 4 + bt); SB(); }
        }
    u32x4 R[4], Rn[4];
#pragma unroll
    for (int mt = 0; mt < 4; ++mt) {
        f32x4 zr = {0.f, 0.f, 0.f, 0.f}, zi = {0.f, 0.f, 0.f, 0.f};
#pragma unroll
        for (int bt = 0; bt < 4; ++bt) { zr = mfma(A3[mt][bt], P[bt], zr); zi = mfma(A3[mt][bt], Pn[bt], zi); }
        if (!L) { w1f_dma(rws, lb, m1, w, lane, 8 + 2 * mt); w1f_dma(rws, lb, m1, w, lane, 9 + 2 * mt); SB(); }
        unsigned pr[4], pi[4];
#pragma unroll
        for (int r = 0; r < 4; ++r) {
            const float fr = blo(wf[mt][r]), fi = bhi(wf[mt][r]);
            const float yr = zr[r] * fr - zi[r] * fi, yi = zr[r] * fi + zi[r] * fr;
            pr[r] = pk(yr, -yi); pi[r] = pk(yi, yr);
        }
        R[mt] = (u32x4){pr[0], pr[1], pr[2], pr[3]}; Rn[mt] = (u32x4){pi[0], pi[1], pi[2], pi[3]};
    }

    SB();
    unsigned* TR = (unsigned*)smem;
#pragma unroll
    for (int bt = 0; bt < 4; ++bt) {
        f32x4 ur = {0.f, 0.f, 0.f, 0.f}, ui = {0.f, 0.f, 0.f, 0.f};
#pragma unroll
        for (int kt = 0; kt < 4; ++kt) { ur = mfma(A3[bt][kt], R[kt], ur); ui = mfma(A3[bt][kt], Rn[kt], ui); }
        if constexpr (!L) {
#pragma unroll
            for (int i = 16 + 4 * bt; i < 20 + 4 * bt && i < 30; ++i) w1f_dma(rws, lb, m1, w, lane, i);
            SB();
        }
#pragma unroll
        for (int r2 = 0; r2 < 2; ++r2) {
            const f32x2 a = {ur[2 * r2], ur[2 * r2 + 1]}, b = {ui[2 * r2], ui[2 * r2 + 1]};
            const f32x2 xr = a * twc[bt][r2] - b * tws[bt][r2], xi = b * twc[bt][r2] + a * tws[bt][r2];
#pragma unroll
            for (int e = 0; e < 2; ++e) {
                const int r = 2 * r2 + e;
                const int b_ = 16 * bt + 4 * q + r;
                const int kq = 4 * kh + (n >> 2), f = (2 * q + r2) & 7;
                TR[c * 2048 + b_ * 32 + ((kq ^ f) << 2) + (n & 3)] = pk(xr[e], xi[e]);
            }
        }
        SB();
    }
    bar();

    {
        const int h = w & 1, ah = w >> 1;
        const u32x4* TRv = (const u32x4*)smem;
        u32x4 bq[2][2][2], raw[2][2];
#pragma unroll
        for (int hf = 0; hf < 2; ++hf)
#pragma unroll
            for (int cc = 0; cc < 2; ++cc) {
                const int b_ = 16 * (2 * h + hf) + n, f = (n >> 1) & 7;
                bq[hf][cc][0] = TRv[cc * 512 + b_ * 8 + ((0 + q) ^ f)]; bq[hf][cc][1] = TRv[cc * 512 + b_ * 8 + ((4 + q) ^ f)];
            }
#pragma unroll
        for (int rt = 0; rt < 2; ++rt)
#pragma unroll
            for (int e = 0; e < 2; ++e) raw[rt][e] = RAW[((16 * ah + 8 * rt + 2 * q + e) * 2 + h) * 16 + n];
        SB();
        u32x4 o[2][2];
#pragma unroll
        for (int rt = 0; rt < 2; ++rt) {
#pragma unroll
            for (int hf = 0; hf < 2; ++hf)
#pragma unroll
                for (int cc = 0; cc < 2; ++cc) {
                    const unsigned r0 = raw[rt][0][hf * 2 + cc], r1 = raw[rt][1][hf * 2 + cc];
                    f32x4 y = {0.f, 0.f, 0.f, 0.f};
                    y = mfma(A6[rt][0], bq[hf][cc][0], y); y = mfma(A6[rt][1], bq[hf][cc][1], y);
                    if (cc == 0 && g == 0) { y[0] = 0.f; y[2] = 0.f; }
                    o[rt][0][hf * 2 + cc] = pk(y[0] + blo(r0), y[1] + bhi(r0));
                    o[rt][1][hf * 2 + cc] = pk(y[2] + blo(r1), y[3] + bhi(r1));
                }
#pragma unroll
            for (int e = 0; e < 2; ++e)
                cst16(rws, o_out + g * GS + (2 * (16 * ah + 8 * rt + 2 * q + e) + h) * 256u + n * 16u, o[rt][e]);
        }
    }

    publish(p.ws, L ? PH_C2 : PH_C1, g, ep);

}

#define WM0 40
#define WG0 20
#define WM1 25
#define WG1 20
template <int L>
DI void phase_g1(const Params& p, int tt, int fs, int rb, int ct, u32x4 (&W)[5][12], unsigned wo, u32x4 (&xg)[2][5][2], u32x4 (&QT)[5][5], u64 ep, char* smem) {
    constexpr unsigned o_in = L ? O_XH3 : O_XH1, o_hf = L ? O_HF2 : O_HF1;
    constexpr int SO = L ? 36 : 17;
    constexpr int DP = 8;
    IDS;
    const rsrc_t rws = mkrs(p.ws, WS_USED), rb1 = mkrs(p.b1, F * 2);
    u32x2 bb[6];
#pragma unroll
    for (int mt = 0; mt < 6; ++mt) bb[mt] = ld8(rb1, (96 * fs + 16 * mt + 4 * q) * 2u);
    {
        int e = (L ? PH_C2 : PH_C1) * 256 + w * 40 + (lane < 40 ? lane : 0);
        if (!L && lane >= 40) {
            const int cg = (lane & 1) ? (80 * ct + 79) >> 7 : (80 * ct) >> 7;
            e = PH_A2 * 256 + (lane < 48 ? 5 * (4 * w + ((lane >> 1) & 3)) + cg : 80 + cg);
        }
        if (L && lane >= 40) e = PH_A3 * 256 + 2 * (12 * w + (lane - 40) % 12) + (ct >> 2);
        if constexpr (L) {
            const u64 fv = peek_flag(p.ws, 0, e);
            SB();
            gen_qt(QT, ct, w, lane);
            finish_wait(p.ws, 0, e, ep, fv);
        } else wait_flags(p.ws, 0, e, ep);
    }
    bar();

    const int blk = 4 * tt + w;
    const unsigned xo = o_in + q * GS + blk * 256u + n * 16u;
    const unsigned go = O_XH3 + (40 * w + q) * GS + (2 * rb) * 256u + n * 16u;
    u32x4 l[DP][2];
#pragma unroll
    for (int kk = 0; kk < DP; ++kk)
#pragma unroll
        for (int e = 0; e < 2; ++e) l[kk][e] = cld16(rws, xo + (8 * kk + 4 * e) * GS);
    SB();
    const u32x4* WL = (const u32x4*)smem + lane;
    f32x4 acc[6][2], gr0 = {0.f, 0.f, 0.f, 0.f}, gr1 = {0.f, 0.f, 0.f, 0.f};
#pragma unroll
    for (int mt = 0; mt < 6; ++mt) { acc[mt][0] = (f32x4){0.f, 0.f, 0.f, 0.f}; acc[mt][1] = (f32x4){0.f, 0.f, 0.f, 0.f}; }
    u32x4 A[2][6];
#pragma unroll
    for (int mt = 0; mt < 6; ++mt) A[0][mt] = WL[(mt * 20) * 64];
    u32x4 f0 = {l[0][0][0], l[0][0][1], l[0][1][0], l[0][1][1]}, f1 = {l[0][0][2], l[0][0][3], l[0][1][2], l[0][1][3]};
    if (q == 0) { f0[0] &= 0xffff0000u; f1[0] &= 0xffff0000u; }
    SB();
#pragma unroll
    for (int kk = 0; kk < 20; ++kk) {
        u32x4 g0, g1;
#pragma unroll
        for (int mt = 0; mt < 6; ++mt) {
            acc[mt][0] = mfma(A[kk & 1][mt], f0, acc[mt][0]);
            acc[mt][1] = mfma(A[kk & 1][mt], f1, acc[mt][1]);
            if (kk < 19) A[(kk + 1) & 1][mt] = WL[(mt * 20 + kk + 1) * 64];
            if (mt == 0) gr0 = mfma(f0, f0, gr0);
            if (mt == 1) gr1 = mfma(f1, f1, gr1);
            if (mt == 2 && kk < 19) {
                const int s = (kk + 1) % DP;
                g0 = (u32x4){l[s][0][0], l[s][0][1], l[s][1][0], l[s][1][1]};
                g1 = (u32x4){l[s][0][2], l[s][0][3], l[s][1][2], l[s][1][3]};
            }
            if ((mt == 0 || mt == 3) && kk + DP < 20) l[kk % DP][mt ? 1 : 0] = cld16(rws, xo + (8 * (kk + DP) + 4 * (mt ? 1 : 0)) * GS);
            if (mt == 4) {
                constexpr int WM = L ? WM1 : WM0;
#pragma unroll
                for (int i = kk * WM / 20; i < (kk + 1) * WM / 20; ++i) W[i % 5][i / 5] = cld16(rws, wo + ((i % 5) * 48 + i / 5) * 1024u);
            }
            if (L && mt == 5) xg[kk / 10][(kk % 10) >> 1][kk & 1] = cld16(rws, go + (8 * ((kk % 10) >> 1) + 4 * (kk & 1)) * GS + (((kk / 10) ^ (w >> 1)) * 256u));
            SB();
        }
        if (kk < 19) { f0 = g0; f1 = g1; }
        }

    float rs[2];
    {
        const int r = n & 3;
        const float s0 = r == 0 ? gr0[0] : r == 1 ? gr0[1] : r == 2 ? gr0[2] : gr0[3];
        const float s1 = r == 0 ? gr1[0] : r == 1 ? gr1[1] : r == 2 ? gr1[2] : gr1[3];
        const bool dg = q == (n >> 2);
        rs[0] = __builtin_amdgcn_rsqf(__builtin_fmaf(sumq(dg ? s0 : 0.f), 1.f / 640.f, 1e-5f));
        rs[1] = __builtin_amdgcn_rsqf(__builtin_fmaf(sumq(dg ? s1 : 0.f), 1.f / 640.f, 1e-5f));
    }
    constexpr float C0 = -2.3022081985f, C1 = -0.1029432397f;
    constexpr int WM = L ? WM1 : WM0, WG = L ? WG1 : WG0;
    SB();
#pragma unroll
    for (int u = 0; u < 3; ++u) {
        f32x2 bv[4];
#pragma unroll
        for (int i = 0; i < 4; ++i) bv[i] = (f32x2){blo(bb[2 * u + (i >> 1)][i & 1]), bhi(bb[2 * u + (i >> 1)][i & 1])};
#pragma unroll
        for (int hf = 0; hf < 2; ++hf) {
            const int gi = 2 * (2 * u + hf);
            f32x2 x[4], t[4];
#pragma unroll
            for (int i = 0; i < 4; ++i) {
                const f32x4 a = acc[2 * u + (i >> 1)][hf];
                x[i] = __builtin_elementwise_fma((f32x2){a[2 * (i & 1)], a[2 * (i & 1) + 1]}, (f32x2){rs[hf], rs[hf]}, bv[i]);
            }
#pragma unroll
            for (int i = 0; i < 4; ++i) t[i] = x[i] * x[i];
#pragma unroll
            for (int i = 0; i < 4; ++i) t[i] = __builtin_elementwise_fma(t[i], (f32x2){C1, C1}, (f32x2){C0, C0});
#pragma unroll
            for (int i = 0; i < 4; ++i) t[i] = x[i] * t[i];
            SB();
#pragma unroll
            for (int i = WM + gi * WG / 12; i < WM + (gi + 1) * WG / 12; ++i) W[i % 5][i / 5] = cld16(rws, wo + ((i % 5) * 48 + i / 5) * 1024u);
#pragma unroll
            for (int i = 0; i < 4; ++i) t[i] = (f32x2){__builtin_amdgcn_exp2f(t[i][0]), __builtin_amdgcn_exp2f(t[i][1])};
            SB();
#pragma unroll
            for (int i = 0; i < 4; ++i) t[i] = t[i] + 1.f;
            SB();
#pragma unroll
            for (int i = WM + (gi + 1) * WG / 12; i < WM + (gi + 2) * WG / 12; ++i) W[i % 5][i / 5] = cld16(rws, wo + ((i % 5) * 48 + i / 5) * 1024u);
#pragma unroll
            for (int i = 0; i < 4; ++i) t[i] = (f32x2){__builtin_amdgcn_rcpf(t[i][0]), __builtin_amdgcn_rcpf(t[i][1])};
            SB();
            u32x4 o;
#pragma unroll
            for (int i = 0; i < 4; ++i) x[i] = x[i] * t[i];
#pragma unroll
            for (int i = 0; i < 4; ++i) o[i] = pk(x[i][0], x[i][1]);
            cst16(rws, o_hf + (((2 * blk + hf) * 48 + 3 * fs + u) * 64 + lane) * 16u, o);
            SB();
        }
    }

    publish(p.ws, L ? PH_G1B : PH_G1A, tt * 16 + fs, ep);

}

#define HSTREAM(o_hf, ph, stamp) \
    wait_flags(p.ws, ph, (rb >> 1) * 16 + 4 * w + (lane & 3), ep); \
 \
    u32x4 hb[3][4]; \
    const unsigned ho = o_hf + ((4 * rb * 48 + 12 * w) * 64 + lane) * 16u; \
    _Pragma("unroll") for (int u = 0; u < 3; ++u) { _Pragma("unroll") for (int j = 0; j < 4; ++j) hb[u][j] = cld16(rws, ho + ((j ^ w) * 48 + u) * 1024u); } \
    SB()

DI void phase_g2(const Params& p, int rb, int ct, u32x4 (&W)[5][12], unsigned wo, u64 ep, char* scr) {
    IDS;
    const rsrc_t rws = mkrs(p.ws, WS_USED);
#pragma unroll
    for (int i = WM0 + WG0; i < 60; ++i) W[i % 5][i / 5] = cld16(rws, wo + ((i % 5) * 48 + i / 5) * 1024u);
    SB();
    HSTREAM(O_HF1, PH_G1A, 21);
    f32x4 acc[4][5];
    const f32x4 z = {0.f, 0.f, 0.f, 0.f};
    u32x2 res[5]; f32x4 bv[5];
#pragma unroll
    for (int u = 0; u < 10; ++u) {
#pragma unroll
        for (int j = 0; j < 4; ++j) {
#pragma unroll
            for (int mt = 0; mt < 5; ++mt) acc[j][mt] = mfma(W[mt][u], hb[u % 3][j], u == 0 ? z : acc[j][mt]);
            if (u + 3 < 12) hb[u % 3][j] = cld16(rws, ho + ((j ^ w) * 48 + u + 3) * 1024u);
            SB();
        }
        if (u == 8) {
#pragma unroll
            for (int mt = 0; mt < 5; ++mt) {
                res[mt] = cld8(rws, O_XH1 + (20 * ct + 4 * mt + q) * GS + (2 * rb + (w >> 1)) * 256u + n * 16u + (w & 1) * 8u);
                bv[mt] = __builtin_bit_cast(f32x4, cld16(rws, O_B2P + (80 * ct + 16 * mt + 4 * q) * 4u));
            }
            SB();
        }
        }
    ktail<5, 640>(acc, (f32x4*)scr, w, lane, [&](int j, int part) {
#pragma unroll
        for (int mt = 0; mt < 5; ++mt) acc[j][mt] = mfma(W[mt][10 + part], hb[(10 + part) % 3][j], acc[j][mt]);
    });

#pragma unroll
    for (int mt = 0; mt < 5; ++mt) {
        acc[0][mt] += bv[mt];
        acc[0][mt][0] += blo(res[mt][0]); acc[0][mt][1] += bhi(res[mt][0]); acc[0][mt][2] += blo(res[mt][1]); acc[0][mt][3] += bhi(res[mt][1]);
    }
    store_xh(acc[0], rws, O_XH2, O_ST2, rb, ct, w, lane, scr);

    publish(p.ws, PH_G2, rb * 8 + ct, ep);

}

DI void phase_g4(const Params& p, int rb, int ct, u32x4 (&W)[5][12], unsigned wo, const u32x4 (&QT)[5][5], const u32x4 (&xg)[2][5][2], u64 ep, char* scr) {
    IDS;
    const rsrc_t rws = mkrs(p.ws, WS_USED), rb2 = mkrs(p.b2, D * 2), rout = mkrs(p.out, S * D * 2);
    f32x4 acc[4][5];
    const f32x4 z = {0.f, 0.f, 0.f, 0.f};
    const bool odd = w & 1;
#pragma unroll
    for (int b = 0; b < 2; ++b)
#pragma unroll
        for (int kk = 0; kk < 5; ++kk) {
            const u32x4 x0 = xg[b][kk][0], x1 = xg[b][kk][1];
            const u32x4 lo = {x0[0], x0[1], x1[0], x1[1]}, hi = {x0[2], x0[3], x1[2], x1[3]};
            const u32x4 fa = odd ? hi : lo, fb = odd ? lo : hi;
#pragma unroll
            for (int mt = 0; mt < 5; ++mt) acc[2 * b][mt] = mfma(QT[mt][kk], fa, kk == 0 ? z : acc[2 * b][mt]);
#pragma unroll
            for (int mt = 0; mt < 5; ++mt) acc[2 * b + 1][mt] = mfma(QT[mt][kk], fb, kk == 0 ? z : acc[2 * b + 1][mt]);
            const int s = b * 5 + kk;
#pragma unroll
            for (int i = WM1 + WG1 + (60 - WM1 - WG1) * s / 10; i < WM1 + WG1 + (60 - WM1 - WG1) * (s + 1) / 10; ++i) W[i % 5][i / 5] = cld16(rws, wo + ((i % 5) * 48 + i / 5) * 1024u);
            SB();
        }

    HSTREAM(O_HF2, PH_G1B, 41);
    u32x2 bb[5];
#pragma unroll
    for (int u = 0; u < 10; ++u) {
#pragma unroll
        for (int j = 0; j < 4; ++j) {
#pragma unroll
            for (int mt = 0; mt < 5; ++mt) acc[j][mt] = mfma(W[mt][u], hb[u % 3][j], acc[j][mt]);
            if (u + 3 < 12) hb[u % 3][j] = cld16(rws, ho + ((j ^ w) * 48 + u + 3) * 1024u);
            SB();
        }
        if (u == 8) {
#pragma unroll
            for (int mt = 0; mt < 5; ++mt) bb[mt] = ld8(rb2, (80 * ct + 16 * mt + 4 * q) * 2u);
            SB();
        }
        }
    ktail<5, 640>(acc, (f32x4*)scr, w, lane, [&](int j, int part) {
#pragma unroll
        for (int mt = 0; mt < 5; ++mt) acc[j][mt] = mfma(W[mt][10 + part], hb[(10 + part) % 3][j], acc[j][mt]);
    });

#pragma unroll
    for (int mt = 0; mt < 5; ++mt) {
        const unsigned d0 = pk(acc[0][mt][0] + blo(bb[mt][0]), acc[0][mt][1] + bhi(bb[mt][0]));
        const unsigned d1 = pk(acc[0][mt][2] + blo(bb[mt][1]), acc[0][mt][3] + bhi(bb[mt][1]));
        st8(rout, (64 * rb + 16 * w + n) * 1280u + (80 * ct + 16 * mt + 4 * q) * 2u, (u32x2){d0, d1});
    }

}

__global__ void __launch_bounds__(256, 1) k_all(Params p) {
    __shared__ __attribute__((aligned(16))) char smem[163840];
    char* scr = smem + LDS_SCR;

    unsigned pf_code;
    {
        u64 pc; unsigned len;
        asm volatile("s_getpc_b64 %0\nkall_pf_begin:\n\ts_mov_b32 %1, kall_pf_end-kall_pf_begin" : "=s"(pc), "=s"(len));
        pf_code = ld4(mkrs((const void*)pc, len), (blockIdx.x >> 3) * 4096u + threadIdx.x * 16u);
    }
#define MAP const int id = fresh_s(blockIdx.x); const Map mp = get_map(id)
    GAS u64* epw = (GAS u64*)(p.ws + O_EPO);
    const u64 ep_in = __hip_atomic_load(epw, __ATOMIC_RELAXED, __HIP_MEMORY_SCOPE_AGENT);

    u64 ep;
    {
        MAP;

        if (mp.isC) {
            phase_p0(p, mp.g, smem);
            ep = rfl64(ep_in) + 1;
            publish(p.ws, PH_P0, mp.g, ep);

            phase_c<0>(p, mp.g, mp.m, ep, smem, scr);
        } else {
            phase_aux(p, mp.ar, mp.m, ep_in, ep, smem);
        }
        PIN(pf_code);
    }

    u32x4 W[5][12], QT[5][5], xg[2][5][2];
    {
        MAP; IDS;
        const unsigned wo = O_W2F + ((5 * mp.ct * 48 + 12 * w) * 64 + lane) * 16u;
        phase_g1<0>(p, mp.tt, mp.m, mp.rb, mp.ct, W, wo, xg, QT, ep, smem);
        phase_g2(p, mp.rb, mp.ct, W, wo, ep, scr);
    }

    {
        MAP;
        if (mp.isC) phase_c<1>(p, mp.g, mp.m, ep, smem, scr);
    }
    {
        MAP; IDS;

        const unsigned wo = O_W2T + ((5 * mp.ct * 48 + 12 * w) * 64 + lane) * 16u;
        phase_g1<1>(p, mp.tt, mp.m, mp.rb, mp.ct, W, wo, xg, QT, ep, smem);
        phase_g4(p, mp.rb, mp.ct, W, wo, QT, xg, ep, scr);
    }
    if (blockIdx.x == 0 && threadIdx.x == 0) __hip_atomic_store(epw, ep, __ATOMIC_RELAXED, __HIP_MEMORY_SCOPE_AGENT);

    asm volatile("kall_pf_end:");
}

extern "C" void kernel_launch(void* const* d_in, const int* in_sizes, int n_in, void* d_out, int out_size, void* d_ws, size_t ws_size,
                              hipStream_t stream) {
    Params p;
    p.x = (const uint16_t*)d_in[0]; p.wr = (const uint16_t*)d_in[1]; p.wi = (const uint16_t*)d_in[2];
    p.w1 = (const uint16_t*)d_in[4]; p.b1 = (const uint16_t*)d_in[5]; p.w2 = (const uint16_t*)d_in[6]; p.b2 = (const uint16_t*)d_in[7];
    p.out = (uint16_t*)d_out; p.ws = (char*)d_ws;
    k_all<<<256, 256, 0, stream>>>(p);
}
```

```cpp
#include <hip/hip_runtime.h>
#include <stdint.h>
#include <utility>

#pragma clang diagnostic ignored "-Winline-asm"
typedef float f32x4 __attribute__((ext_vector_type(4)));
typedef float f32x2 __attribute__((ext_vector_type(2)));
typedef unsigned u32x4 __attribute__((ext_vector_type(4)));
typedef unsigned u32x2 __attribute__((ext_vector_type(2)));
typedef __bf16 bf16x8 __attribute__((ext_vector_type(8)));
typedef __bf16 bf16x2 __attribute__((ext_vector_type(2)));
typedef __amdgpu_buffer_rsrc_t rsrc_t;
typedef unsigned long long u64;
#define DI __device__ __forceinline__
#define GAS __attribute__((address_space(1)))
#define LAS __attribute__((address_space(3)))

constexpr int S = 2048, D = 640, F = 1536;
#define GSPAD 0
constexpr unsigned GS = 16384u + GSPAD;
constexpr unsigned XH_BYTES = 160u * GS;
constexpr unsigned O_XH0 = 0, O_XH1 = XH_BYTES, O_XH2 = 2 * XH_BYTES, O_XH3 = 3 * XH_BYTES;
constexpr unsigned O_HF1 = 0x0B00000, O_HF2 = 0x1100000;
constexpr unsigned O_W1F = 0x1700000;
constexpr unsigned O_W2F = 0x1900000;
constexpr unsigned O_W2T = 0x1B00000;
constexpr unsigned O_WFQ = 0x1D00000;
constexpr unsigned O_ST0 = 0x1F80000, O_ST2 = 0x1F90000;
#define NREP 4
#define FSTR 128
constexpr unsigned O_FLG = 0x2200000;
constexpr unsigned FREP = 9 * 256 * FSTR + 0x8000;
constexpr unsigned O_EPO = 0x1FF0000;
constexpr unsigned O_B2P = 0x20D0000;
constexpr unsigned WS_USED = 0x2100000;
constexpr unsigned LDS_SCR = 122880;
enum { PH_P0 = 0, PH_A1, PH_A2, PH_A3, PH_C1, PH_G1A, PH_G2, PH_C2, PH_G1B };

struct Params {
    const uint16_t* x; const uint16_t* wr; const uint16_t* wi; const uint16_t* w1; const uint16_t* b1;
    const uint16_t* w2; const uint16_t* b2; uint16_t* out; char* ws;
};

#define PIN(x) asm volatile("" : "+v"(x))
#define A3N 3
#define PINA(x) PIN(x)

DI rsrc_t mkrs(const void* p, unsigned bytes) { return __builtin_amdgcn_make_buffer_rsrc((void*)p, 0, bytes, 0x00020000); }
DI u32x4 ld16(rsrc_t r, unsigned off) { return __builtin_amdgcn_raw_buffer_load_b128(r, off, 0, 0); }
DI u32x2 ld8(rsrc_t r, unsigned off) { return __builtin_amdgcn_raw_buffer_load_b64(r, off, 0, 0); }
DI unsigned ld4(rsrc_t r, unsigned off) { return __builtin_amdgcn_raw_buffer_load_b32(r, off, 0, 0); }
DI unsigned ld2(rsrc_t r, unsigned off) { return __builtin_amdgcn_raw_buffer_load_b16(r, off, 0, 0); }
DI void st16(rsrc_t r, unsigned off, u32x4 v) { __builtin_amdgcn_raw_buffer_store_b128(v, r, off, 0, 0); }
DI void st8(rsrc_t r, unsigned off, u32x2 v) { __builtin_amdgcn_raw_buffer_store_b64(v, r, off, 0, 0); }
DI u32x4 cld16(rsrc_t r, unsigned off) { return __builtin_amdgcn_raw_buffer_load_b128(r, off, 0, 16); }
DI u32x2 cld8(rsrc_t r, unsigned off) { return __builtin_amdgcn_raw_buffer_load_b64(r, off, 0, 16); }
DI void cst16(rsrc_t r, unsigned off, u32x4 v) { __builtin_amdgcn_raw_buffer_store_b128(v, r, off, 0, 16); }
DI void cst8(rsrc_t r, unsigned off, u32x2 v) { __builtin_amdgcn_raw_buffer_store_b64(v, r, off, 0, 16); }
DI void cst4(rsrc_t r, unsigned off, unsigned v) { __builtin_amdgcn_raw_buffer_store_b32(v, r, off, 0, 16); }
DI void dma16(rsrc_t r, unsigned voff, unsigned soff, unsigned lds) {
    asm volatile("s_mov_b32 m0, %0\n\ts_nop 0\n\tbuffer_load_dwordx4 %1, %2, %3 offen lds" :: "s"(lds), "v"(voff), "s"(r), "s"(soff) : "m0", "memory");
}
DI void cdma16(rsrc_t r, unsigned voff, unsigned soff, unsigned lds) {
    asm volatile("s_mov_b32 m0, %0\n\ts_nop 0\n\tbuffer_load_dwordx4 %1, %2, %3 offen sc1 lds" :: "s"(lds), "v"(voff), "s"(r), "s"(soff) : "m0", "memory");
}
#define DMA_HEAD "s_add_u32 m0, %1, %5\n\ts_add_u32 %0, %2, %5\n\t"
#define DMA_LD(k, sc) "buffer_load_dwordx4 %3, %4, %0 offen offset:%c6+" #k sc " lds\n\t"
template <int A, int B0, int N>
DI void cdma_run(rsrc_t r, unsigned voff, unsigned sg, unsigned sl) {
    unsigned t;
    if constexpr (N == 1) asm volatile(DMA_HEAD DMA_LD(0, " sc1") : "=&s"(t) : "s"(sl), "s"(sg), "v"(voff), "s"(r), "n"(A), "n"(B0 * 1024) : "m0", "scc", "memory");
    if constexpr (N == 2) asm volatile(DMA_HEAD DMA_LD(0, " sc1") DMA_LD(1024, " sc1") : "=&s"(t) : "s"(sl), "s"(sg), "v"(voff), "s"(r), "n"(A), "n"(B0 * 1024) : "m0", "scc", "memory");
    if constexpr (N == 3) asm volatile(DMA_HEAD DMA_LD(0, " sc1") DMA_LD(1024, " sc1") DMA_LD(2048, " sc1") : "=&s"(t) : "s"(sl), "s"(sg), "v"(voff), "s"(r), "n"(A), "n"(B0 * 1024) : "m0", "scc", "memory");
    if constexpr (N == 4) asm volatile(DMA_HEAD DMA_LD(0, " sc1") DMA_LD(1024, " sc1") DMA_LD(2048, " sc1") DMA_LD(3072, " sc1") : "=&s"(t) : "s"(sl), "s"(sg), "v"(voff), "s"(r), "n"(A), "n"(B0 * 1024) : "m0", "scc", "memory");
}
template <int B0>
DI void dma_pair(rsrc_t r, unsigned v0, unsigned v1, unsigned soff, unsigned lds) {
    asm volatile("s_mov_b32 m0, %0\n\ts_nop 0\n\tbuffer_load_dwordx4 %1, %3, %4 offen offset:%c5 lds\n\tbuffer_load_dwordx4 %2, %3, %4 offen offset:%c6 lds"
                 :: "s"(lds), "v"(v0), "v"(v1), "s"(r), "s"(soff), "n"(B0 * 1024), "n"(B0 * 1024 + 1024) : "m0", "memory");
}
#define VMWAIT(n) asm volatile("s_waitcnt vmcnt(" #n ")" ::: "memory")
#define VMBAR(n) asm volatile("s_waitcnt vmcnt(" #n ")\n\ts_barrier" ::: "memory")
#define VMLBAR(n) asm volatile("s_waitcnt vmcnt(" #n ") lgkmcnt(0)\n\ts_barrier" ::: "memory")
typedef short s16x4 __attribute__((ext_vector_type(4)));
DI u32x2 trd(const char* a) { return __builtin_bit_cast(u32x2, __builtin_amdgcn_ds_read_tr16_b64_v4i16((LAS s16x4*)a)); }
DI f32x4 mfma(u32x4 a, u32x4 b, f32x4 c) {
    return __builtin_amdgcn_mfma_f32_16x16x32_bf16(__builtin_bit_cast(bf16x8, a), __builtin_bit_cast(bf16x8, b), c, 0, 0, 0);
}
DI unsigned pk(float lo, float hi) { bf16x2 v = __builtin_convertvector((f32x2){lo, hi}, bf16x2); return __builtin_bit_cast(unsigned, v); }
DI void swp(u32x4& a, int i, u32x4& b, int j) {
    unsigned x = a[i], y = b[j];
    asm("v_swap_b32 %0, %1" : "+v"(x), "+v"(y));
    a[i] = x; b[j] = y;
}
DI float blo(unsigned u) { return __uint_as_float(u << 16); }
DI float bhi(unsigned u) { return __uint_as_float(u & 0xffff0000u); }
DI float dot2(unsigned a, unsigned b, float c) { return __builtin_amdgcn_fdot2_f32_bf16(__builtin_bit_cast(bf16x2, a), __builtin_bit_cast(bf16x2, b), c, false); }
DI float vcos(float r) { return __builtin_amdgcn_cosf(r); }
DI float vsin(float r) { return __builtin_amdgcn_sinf(r); }
DI float fract(float r) { return __builtin_amdgcn_fractf(r); }
#define SB() __builtin_amdgcn_sched_barrier(0)
DI void bar() { asm volatile("s_waitcnt lgkmcnt(0)\n\ts_barrier" ::: "memory"); }
DI float sumq(float v) {
    auto a = __builtin_amdgcn_permlane16_swap(__float_as_uint(v), __float_as_uint(v), false, false);
    v = __uint_as_float(a[0]) + __uint_as_float(a[1]);
    auto b = __builtin_amdgcn_permlane32_swap(__float_as_uint(v), __float_as_uint(v), false, false);
    return __uint_as_float(b[0]) + __uint_as_float(b[1]);
}
template <class F, int... I> DI void sfor_(F f, std::integer_sequence<int, I...>) { (f(std::integral_constant<int, I>{}), ...); }
template <int N, class F> DI void sfor(F f) { sfor_(f, std::make_integer_sequence<int, N>{}); }
DI int fresh_tid() { int t = threadIdx.x; asm volatile("" : "+v"(t)); return t; }
constexpr int imin(int a, int b) { return a < b ? a : b; }
DI unsigned rfl(unsigned v) { return (unsigned)__builtin_amdgcn_readfirstlane((int)v); }
DI u64 rfl64(u64 v) { return (u64)rfl((unsigned)(v >> 32)) << 32 | (u64)rfl((unsigned)v); }
DI unsigned csel(unsigned a, unsigned b, unsigned long long m) { unsigned d; asm volatile("v_cndmask_b32 %0, %1, %2, %3" : "=v"(d) : "v"(a), "v"(b), "s"(m)); return d; }
DI int fresh_s(int v) { asm volatile("" : "+s"(v)); return v; }
#define IDS const int tid = fresh_tid(), w = __builtin_amdgcn_readfirstlane(tid >> 6), lane = tid & 63, n = lane & 15, q = lane >> 4

DI int a2_idx(int ct, int w, int lane) {
    const int l = lane & 15, cg = (l & 1) ? (80 * ct + 79) >> 7 : (80 * ct) >> 7;
    return l < 8 ? 5 * (4 * w + (l >> 1)) + cg : 80 + cg;
}
DI GAS u64* flag_ptr(char* ws, int e, int r) { return (GAS u64*)(ws + O_FLG + r * FREP + e * FSTR); }
#define RSH 0
DI GAS u64* flag_ptr(char* ws, int e) { return flag_ptr(ws, e, (blockIdx.x >> RSH) & (NREP - 1)); }
template <int N = 0>
DI void publish(char* ws, int ph, int idx, u64 ep) {
    asm volatile("s_waitcnt vmcnt(%0)" :: "n"(N) : "memory");
    bar();
    if (threadIdx.x < NREP) __hip_atomic_store(flag_ptr(ws, ph * 256 + idx, threadIdx.x), ep, __ATOMIC_RELAXED, __HIP_MEMORY_SCOPE_AGENT);
}
#define POLL 0
#define PSL 1
DI void wait_flags(char* ws, int ph, int idx, u64 ep) {
    GAS u64* f = flag_ptr(ws, ph * 256 + idx);
#pragma unroll 1
    for (int s = 0; s < 400000; ++s) {
        const u64 v = __hip_atomic_load(f, __ATOMIC_RELAXED, __HIP_MEMORY_SCOPE_AGENT);
        if (__all(v == ep)) break;
        __builtin_amdgcn_s_sleep(PSL);
    }
    asm volatile("" ::: "memory");
}

DI u64 peek_flag(char* ws, int ph, int idx) {
    return __hip_atomic_load(flag_ptr(ws, ph * 256 + idx), __ATOMIC_RELAXED, __HIP_MEMORY_SCOPE_AGENT);
}
DI void finish_wait(char* ws, int ph, int idx, u64 ep, u64 first) {
    if (!__all(first == ep)) wait_flags(ws, ph, idx, ep);
    asm volatile("" ::: "memory");
}

constexpr float AMP = 0.05590169943749474f;
constexpr float AMP0 = 0.03952847075210474f;

struct Map { int tt, m, rb, ct, isC, g, ar; };
__host__ __device__ inline Map get_map(int id) {
    Map r;
    const int xcd = id & 7, slot = id >> 3;
    r.tt = xcd + 8 * (slot >> 4); r.m = slot & 15; r.rb = 2 * r.tt + (r.m >> 3); r.ct = r.m & 7;
    const bool aux = slot >= 24 || (slot >= 16 && ((slot >> 2) & 1) == (xcd & 1));
    r.isC = !aux;
    r.g = 20 * xcd + (slot < 16 ? slot : 16 + (slot & 3));
    r.ar = 12 * xcd + (slot >= 24 ? slot - 24 : 8 + (slot & 3));
    return r;
}

template <class H>
DI void gen_q(u32x4 (&Q)[2][20], int cg, int w, int lane, H hook) {
    const int n = lane & 15, q = lane >> 4;
    f32x2 dl, r, d32, cj, ck;
#pragma unroll
    for (int mt = 0; mt < 2; ++mt) {
        const int c = 128 * cg + 32 * w + 16 * mt + n;
        const float pf = c == 1 ? 320.f : (float)(c >> 1);
        const float ph = ((c & 1) && c != 1) ? 0.25f : 0.f;
        dl[mt] = pf * (1.f / 640.f);
        const float t = pf * (float)(128 * cg + 8 * q);
        const float k = floorf(t * (1.f / 640.f));
        r[mt] = __builtin_fmaf(-k, 640.f, t) * (1.f / 640.f) + ph;
        d32[mt] = fract(pf * 0.05f);
        cj[mt] = 2.f * vcos(dl[mt]);
        ck[mt] = 2.f * vcos(d32[mt]);
    }
    f32x2 v[2][8];
#pragma unroll
    for (int kk = 0; kk < 20; ++kk) {
        hook(kk);
        f32x2 (&x)[8] = v[kk & 1];
        if (kk < 2) {
            const f32x2 r0 = kk ? r + d32 : r, r1 = r0 + dl;
            x[0] = (f32x2){vcos(r0[0]), vcos(r0[1])};
            x[1] = (f32x2){vcos(r1[0]), vcos(r1[1])};
#pragma unroll
            for (int j = 2; j < 8; ++j) x[j] = __builtin_elementwise_fma(cj, x[j - 1], -x[j - 2]);
        } else {
#pragma unroll
            for (int j = 0; j < 8; ++j) x[j] = __builtin_elementwise_fma(ck, v[(kk & 1) ^ 1][j], -x[j]);
        }
#pragma unroll
        for (int mt = 0; mt < 2; ++mt)
            Q[mt][kk] = (u32x4){pk(x[0][mt], x[1][mt]), pk(x[2][mt], x[3][mt]), pk(x[4][mt], x[5][mt]), pk(x[6][mt], x[7][mt])};
        PIN(Q[0][kk]); PIN(Q[1][kk]);
        SB();
    }
}

#define PZ2 1
#define PZP 2
#define MKP 0
#define MKG 120
#define MKC 2
#define PH2 0
#define MKH 1
constexpr int RN = PH2 ? 16 : 12;
#define PZH 0
constexpr unsigned MARK = 0xffffffffu;
DI unsigned unmark(unsigned v) { return v < MARK - 1 ? v : MARK - 1; }
DI unsigned umax3(unsigned a, unsigned b, unsigned c) { const unsigned m = a > b ? a : b; return m > c ? m : c; }
DI unsigned umax4(unsigned m, u32x4 v) { return umax3(umax3(m, v[0], v[1]), v[2], v[3]); }
DI void mark_g2(rsrc_t rws, int rb, int ct, int w, int lane, int i) {
    if (i < 3) {
        const int k = w + 4 * i;
        cst16(rws, k < 10 ? O_XH2 + (20 * ct + 2 * k + (lane >> 5)) * GS + rb * 512u + (lane & 31) * 16u : 0xfffffff0u, (u32x4){MARK, MARK, MARK, MARK});
    } else {
        const unsigned ln = ((lane >> 4) + 2 * ((rb >> 2) & 1)) * 16 + (lane & 15);
        cst4(rws, (w == 0 && lane < 32) ? O_ST2 + ((ct * 4 + (rb >> 3)) * 64 + ln) * 16u + (rb & 3) * 4u : 0xfffffff0u, MARK);
    }
}
DI void mark_g2(rsrc_t rws, int rb, int ct, int w, int lane) {
#pragma unroll
    for (int i = 0; i < 4; ++i) mark_g2(rws, rb, ct, w, lane, i);
}
template <int NW>
DI void mark_h2(rsrc_t rws, unsigned o_hf, int g, int w, int lane, int i) {
    const unsigned x = 4 * g + w + 4 * NW * i;
    cst16(rws, x < 2048 ? o_hf + (((x >> 4) * 48 + 3 * (x & 15) + 2) * 64 + lane) * 16u : 0xfffffff0u, (u32x4){MARK, MARK, MARK, MARK});
}
DI void mark_h(rsrc_t rws, unsigned o_hf, int tt, int fs, int w, int lane) {
    const u32x4 mk = {MARK, MARK, MARK, MARK};
#pragma unroll
    for (int i = 0; i < 6; ++i) cst16(rws, o_hf + (((2 * (4 * tt + w) + (i & 1)) * 48 + 3 * fs + (i >> 1)) * 64 + lane) * 16u, mk);
}

DI void store_xh(const f32x4 (&v)[5], rsrc_t rws, unsigned o_xh, unsigned o_st, int rb, int ct, int t, int lane, char* sb) {
    const int n = lane & 15, q = lane >> 4;
    u32x2 d[5];
    float ss = 0.f;
#pragma unroll
    for (int mt = 0; mt < 5; ++mt) {
        d[mt] = (u32x2){pk(v[mt][0], v[mt][1]), pk(v[mt][2], v[mt][3])};
        if (PZ2) { d[mt][0] = unmark(d[mt][0]); d[mt][1] = unmark(d[mt][1]); }
        const unsigned m0 = (mt == 0 && ct == 0 && q == 0) ? (d[mt][0] & 0xffff0000u) : d[mt][0];
        ss = dot2(m0, m0, ss); ss = dot2(d[mt][1], d[mt][1], ss);
    }
    u32x2* mine = (u32x2*)(sb + (t ^ 2) * 10240) + lane;
    const u32x2* other = (const u32x2*)(sb + (t ^ 3) * 10240) + lane;
    const unsigned base = o_xh + (20 * ct + q) * GS + (2 * rb + (t >> 1)) * 256u + n * 16u;
    if (t & 1) {
        mine[0] = d[0]; mine[64] = d[1]; mine[128] = d[2];
        ss = sumq(ss);
        mine[192] = (u32x2){__float_as_uint(ss), 0u};
        bar();
        const u32x2 a = other[0], b = other[64];
        cst16(rws, base + 12 * GS, (u32x4){a[0], a[1], d[3][0], d[3][1]});
        cst16(rws, base + 16 * GS, (u32x4){b[0], b[1], d[4][0], d[4][1]});
    } else {
        mine[0] = d[3]; mine[64] = d[4];
        ss = sumq(ss);
        bar();
        const u32x2 a = other[0], b = other[64], c = other[128], s1 = other[192];
        cst16(rws, base, (u32x4){d[0][0], d[0][1], a[0], a[1]});
        cst16(rws, base + 4 * GS, (u32x4){d[1][0], d[1][1], b[0], b[1]});
        cst16(rws, base + 8 * GS, (u32x4){d[2][0], d[2][1], c[0], c[1]});
        const unsigned ln = ((t >> 1) + 2 * ((rb >> 2) & 1)) * 16 + n;
        const unsigned so = o_st + ((ct * 4 + (rb >> 3)) * 64 + ln) * 16u + (rb & 3) * 4u;
        cst4(rws, q == 0 ? so : 0xfffffff0u, unmark(pk(ss, __uint_as_float(s1[0]))));
    }
}

DI int w1f_idx(int m, int lane) { return 5 * m + lane % 5; }
#define DRUN 0
struct W1Adr { rsrc_t r; unsigned v, sg, sl; };
DI W1Adr w1f_adr(rsrc_t rws, unsigned lb, int m, int w, int lane) { return {rws, lane * 16u, O_W1F + m * 122880u + w * 30720u, lb + w * 30720u}; }
template <int I0, int I1>
DI void w1f_dma(const W1Adr& a) {
    if constexpr (I0 < I1) {
        if constexpr (DRUN) {
            constexpr int e = imin((I0 | 3) + 1, I1);
            cdma_run<(I0 >> 2) * 4096, I0 & 3, e - I0>(a.r, a.v, a.sg, a.sl);
            w1f_dma<e, I1>(a);
        } else {
            cdma16(a.r, a.v, a.sg + I0 * 1024u, a.sl + I0 * 1024u);
            w1f_dma<I0 + 1, I1>(a);
        }
    }
}

#define P0V 0
constexpr int P0N(int kk) { return P0V == 0 ? (kk < 10 ? 2 * kk : 20) : P0V == 1 ? kk : P0V == 2 ? (kk < 4 ? 2 * kk : kk < 16 ? kk + 4 : 20) : (kk * 4 + 2) / 3 > 20 ? 20 : (kk * 4 + 2) / 3; }
DI unsigned rot5(int a, int b) { const int s = a + b; return s >= 5 ? s - 5 : s; }

DI void phase_p0(const Params& p, int g, int rb2, int ct2, char* smem) {
    IDS;
    const int rb = g / 5, cg = g - 5 * rb;
    const rsrc_t rws = mkrs(p.ws, WS_USED), rx = mkrs(p.x, S * D * 2);
    const unsigned lb = (unsigned)(size_t)(LAS char*)smem;
    const unsigned sw = q * 1280u + ((n ^ q) << 4);
    const rsrc_t rwr = mkrs(p.wr, S * 321 * 2), rwi = mkrs(p.wi, S * 321 * 2);
    const int ks0 = 16 * (rb >> 4) + 32 * (16 * ((rb >> 2) & 3) + 4 * (rb & 3));
    unsigned fr[2][4], fi[2][4], ny[4];
    const unsigned o1 = ((ks0 + 4 * w + q) * 321 + 64 * cg + 2 * n) * 2u;
    const bool nyq = cg == 0 && w == 1;
    const int ks = ks0 + (lane >> 2) + 32 * (lane & 3), kn = (2048 - ks) & 2047;
    const unsigned oa = nyq ? (ks * 321 + 320) * 2u : 0x80000000u, ob = nyq ? (kn * 321 + 320) * 2u : 0x80000000u;
    u32x4 Q[2][20];
    gen_q(Q, cg, w, lane, [&](int kk) {
#pragma unroll
        for (int i = P0N(kk); i < P0N(kk + 1); ++i)
            dma16(rx, sw ^ ((i & 3) << 6), (64 * rb + 16 * w + 4 * (i & 3)) * 1280u + rot5(cg, i >> 2) * 256u, lb + (i >> 2) * 16384u + w * 4096u + (i & 3) * 1024u);
    });

    const f32x4 z = {0.f, 0.f, 0.f, 0.f};
    f32x4 acc[2][4];
    f32x4 gr = z, sr = z;
    unsigned one = 0x3f803f80u;
    PIN(one);
    const u32x4 ones = {one, one, one, one};
    const unsigned rb0 = n * 256u + ((n ^ q) << 4);
    auto rd = [&](int kk, int j) -> u32x4 { return *(const u32x4*)(smem + (rb0 ^ ((kk & 3) << 6)) + (kk >> 2) * 16384 + j * 4096); };
    auto rdo = [&](int kk) -> u32x4 { return *(const u32x4*)(smem + ((rb0 ^ ((kk & 3) << 6)) + w * 4096u) + (kk >> 2) * 16384); };
    VMBAR(16);
    u32x4 xa[2][4], xo[2], fo4;
    const unsigned fo = O_WFQ + ((64 * cg + 2 * n) * 512 + 16 * rb + 4 * w + q) * 16u;
#pragma unroll
    for (int j = 0; j < 4; ++j) xa[0][j] = rd(0, j);
    xo[0] = rdo(0);
    SB();
#pragma unroll
    for (int kk = 0; kk < 20; ++kk) {
        if (kk == 3) { VMBAR(18); }
        if (kk == 7) { VMBAR(22); }
        if (kk == 11) { VMBAR(24); }
        if (kk == 15) { if (PZ2 && MKC == 1) VMBAR(24); else VMBAR(20); }
        SB();
#pragma unroll
        for (int j = 0; j < 4; ++j) {
            acc[0][j] = mfma(Q[0][kk], xa[kk & 1][j], kk == 0 ? z : acc[0][j]);
            if (kk < 19) xa[(kk + 1) & 1][j] = rd(kk + 1, j);
            if (j == 1) gr = mfma(xo[kk & 1], xo[kk & 1], gr);
            if (j == 3) sr = mfma(ones, xo[kk & 1], sr);
            acc[1][j] = mfma(Q[1][kk], xa[kk & 1][j], kk == 0 ? z : acc[1][j]);
            if (kk < 19 && j == 0) xo[(kk + 1) & 1] = rdo(kk + 1);
            if (j >= 2 && kk < 8) {
                const int it = kk >> 2, jj = kk & 3;
                if (j == 2) fr[it][jj] = __builtin_amdgcn_raw_buffer_load_b32(rwr, o1 + it * 64, jj * 32 * 321 * 2, 0);
                else fi[it][jj] = __builtin_amdgcn_raw_buffer_load_b32(rwi, o1 + it * 64, jj * 32 * 321 * 2, 0);
            }
            if (j >= 2 && kk == 8) ny[j - 2] = ld2(rwr, j == 2 ? oa : ob);
            if (j >= 2 && kk == 9) ny[j] = ld2(rwi, j == 2 ? oa : ob);
            if (PZ2 && MKC == 1 && j == 2 && kk >= 11 && kk < 15) mark_g2(rws, rb2, ct2, w, lane, kk - 11);
            if (kk >= 15 && kk < 19 && j == 0) {
                const int it = (kk - 15) >> 1, o = (kk - 15) & 1;
#pragma unroll
                for (int jj = 0; jj < 4; ++jj) fo4[jj] = __builtin_amdgcn_perm(fi[it][jj], fr[it][jj], o ? 0x07060302u : 0x05040100u);
                PIN(fo4);
            }
            if (kk >= 15 && kk < 19 && j == 2) {
                const int it = (kk - 15) >> 1, o = (kk - 15) & 1;
                cst16(rws, (kk == 15 && cg == 0 && n == 0) ? 0xfffffff0u : fo + it * 32 * 8192u + o * 8192u, fo4);
            }
            if (kk == 19 && j == 0) { fo4[0] = pk((blo(ny[0]) + blo(ny[1])) * 0.5f, (blo(ny[2]) - blo(ny[3])) * 0.5f); PIN(fo4[0]); }
            if (kk == 19 && j == 2) cst4(rws, nyq ? O_WFQ + (16 * rb + (lane >> 2)) * 16u + (lane & 3) * 4u : 0xfffffff0u, fo4[0]);
            SB();
        }
    }
    const bool dc = cg == 0 && w == 0 && q == 0;
#pragma unroll
    for (int mt = 0; mt < 2; ++mt)
#pragma unroll
        for (int b = 0; b < 2; ++b) {
            f32x4 v0 = acc[mt][2 * b] * AMP, v1 = acc[mt][2 * b + 1] * AMP;
            if (mt == 0 && dc) { v0[0] *= AMP0 / AMP; v0[1] *= AMP0 / AMP; v1[0] *= AMP0 / AMP; v1[1] *= AMP0 / AMP; }
            cst16(rws, O_XH0 + (32 * cg + 8 * w + 4 * mt + q) * GS + (2 * rb + b) * 256u + n * 16u,
                  (u32x4){pk(v0[0], v0[1]), pk(v0[2], v0[3]), pk(v1[0], v1[1]), pk(v1[2], v1[3])});
        }
    {
        const int r = n & 3;
        const float dg = r == 0 ? gr[0] : r == 1 ? gr[1] : r == 2 ? gr[2] : gr[3];
        const float ss = sumq(q == (n >> 2) ? dg : 0.f), sm = sr[0];
        const float var = fmaxf(__builtin_fmaf(-sm * (1.f / 640.f), sm, ss) * (1.f / 640.f), 0.f);
        const unsigned so = O_ST0 + (((rb >> 3) * 64 + (2 * ((rb >> 2) & 1) + (w >> 1)) * 16 + n) * 2 + (w & 1)) * 16u + (rb & 3) * 4u;
        cst4(rws, (q == 0 && cg == 0) ? so : 0xfffffff0u, __float_as_uint(__builtin_amdgcn_rsqf(var + 1e-5f)));
    }

}

DI void phase_aux(const Params& p, int ar, int tt, int m, int rb, int ct, u64 ep_in, u64& ep, u64& fa, char* smem) {
    IDS;
    const int xcd = ar / 12, a = ar - 12 * xcd;
    const bool act = a < 10;
    const int spare = 2 * xcd + a - 10;
    const int fj = 2 * xcd + (a >= 5), cg = act ? (a >= 5 ? a - 5 : a) : spare;
    const rsrc_t rws = mkrs(p.ws, WS_USED);
    const unsigned lb = (unsigned)(size_t)(LAS char*)smem;
    const f32x4 z = {0.f, 0.f, 0.f, 0.f};
    if (act || spare < 5) {
        const rsrc_t rw1 = mkrs(p.w1, act ? D * F * 2 : 0), rw2 = mkrs(p.w2, act ? D * F * 2 : 0);
        unsigned v1[3];
#pragma unroll
        for (int b = 0; b < 3; ++b) {
            const int ix = 64 * b + lane, rl = ix / 12, cp = ix - 12 * rl;
            v1[b] = rl * 3072u + ((cp ^ ((rl >> 3) << 1)) << 4);
        }
        auto dma1 = [&](int i) {
            const int k = i / 6, u = (i % 6) / 3, b = i % 3;
            dma16(rw1, v1[b], (128 * rot5(cg, k) + 16 * (2 * w + u)) * 3072u + fj * 192u, lb + k * 24576u + (2 * w + u) * 3072u + b * 1024u);
        };
        const unsigned sw = q * 1280u + ((n ^ q) << 4);
        auto dma2 = [&](int s, int t) {
            dma16(rw2, sw ^ (((2 * w + t) & 3) << 6), (96 * fj + 24 * w + 4 * t) * 1280u + rot5(cg, s) * 256u, lb + (s ? (s - 1) * 24576u : 122880u) + w * 6144u + t * 1024u);
        };
        u32x4 Q[2][20];
        gen_q(Q, cg, w, lane, [&](int kk) {
            const int i0 = kk < 16 ? 2 * kk : 16 + kk, i1 = kk < 16 ? 2 * kk + 2 : 17 + kk;
#pragma unroll
            for (int i = i0; i < i1; ++i) { if (i < 30) dma1(i); else dma2(0, i - 30); }
        });

        if (act) {
            const int qq = (lane >> 2) & 3, pp = lane & 3;
            const unsigned tb = (8 * q + qq) * 192u + (pp >> 1) * 16u + (pp & 1) * 8u;
            const unsigned tbe = tb + (q & 1) * 32u, tbo = tb - (q & 1) * 32u;
            auto rd1 = [&](int kk, int ft) -> u32x4 {
                const char* ap = smem + ((ft & 1) ? tbo : tbe) + (kk >> 2) * 24576u;
                const u32x2 lo = trd(ap + (kk & 3) * 6144 + ft * 32), hi = trd(ap + (kk & 3) * 6144 + ft * 32 + 768);
                return (u32x4){lo[0], lo[1], hi[0], hi[1]};
            };
            const unsigned rb0 = n * 256u + ((n ^ q) << 4);
            auto rd2 = [&](int kk, int ft) -> u32x4 {
                const int s = kk >> 2;
                return *(const u32x4*)(smem + ((rb0 ^ ((kk & 3) << 6)) + (s ? (s - 1) * 24576u : 122880u)) + ft * 4096);
            };
            f32x4 acc[2][6];
            u32x4 fa[2][6];
            VMBAR(30);

#pragma unroll
            for (int ft = 0; ft < 6; ++ft) fa[0][ft] = rd1(0, ft);
            SB();
#pragma unroll
            for (int kk = 0; kk < 20; ++kk) {
                if ((kk & 3) == 3) { VMLBAR(24); }
                SB();
#pragma unroll
                for (int ft = 0; ft < 6; ++ft) {
                    acc[0][ft] = mfma(Q[0][kk], fa[kk & 1][ft], kk == 0 ? z : acc[0][ft]);
                    fa[(kk + 1) & 1][ft] = kk < 19 ? rd1(kk + 1, ft) : rd2(0, ft);
                    acc[1][ft] = mfma(Q[1][kk], fa[kk & 1][ft], kk == 0 ? z : acc[1][ft]);
                    if ((kk & 3) == 3 && kk < 19) dma2((kk >> 2) + 1, ft);
                    SB();
                }
            }

            {
                const bool dc = cg == 0 && w == 0 && q == 0;
#pragma unroll
                for (int ft = 0; ft < 6; ++ft) {
                    f32x4 a0 = acc[0][ft] * AMP;
                    const f32x4 a1 = acc[1][ft] * AMP;
                    if (dc) { a0[0] = 0.f; a0[1] *= AMP0 / AMP; }
                    cst16(rws, O_W1F + (((6 * fj + ft) * 20 + 4 * cg + w) * 64 + lane) * 16u, (u32x4){pk(a0[0], a0[1]), pk(a0[2], a0[3]), pk(a1[0], a1[1]), pk(a1[2], a1[3])});
                }
            }
            SB();

#pragma unroll
            for (int kk = 0; kk < 20; ++kk) {
                if (kk == 3) VMBAR(24);
                if (kk == 7) { VMWAIT(0); ep = rfl64(ep_in) + 1; publish(p.ws, PH_A1, 5 * fj + cg, ep); }
                if ((PH2 & 1) && MKH && kk >= 8 && kk < 15) mark_h2<80>(rws, O_HF1, 5 * fj + cg, w, lane, kk - 8);
                SB();
#pragma unroll
                for (int ft = 0; ft < 6; ++ft) {
                    acc[0][ft] = mfma(fa[kk & 1][ft], Q[0][kk], kk == 0 ? z : acc[0][ft]);
                    acc[1][ft] = mfma(fa[kk & 1][ft], Q[1][kk], kk == 0 ? z : acc[1][ft]);
                    if (kk < 19) fa[(kk + 1) & 1][ft] = rd2(kk + 1, ft);
                    SB();
                }
            }

#pragma unroll
            for (int mt = 0; mt < 2; ++mt) {
                const float am = (mt == 0 && cg == 0 && w == 0 && n < 2) ? AMP0 : AMP;
#pragma unroll
                for (int u = 0; u < 3; ++u) {
                    const f32x4 v0 = acc[mt][2 * u] * am, v1_ = acc[mt][2 * u + 1] * am;
                    cst16(rws, O_W2F + (((8 * cg + 2 * w + mt) * 48 + 3 * fj + u) * 64 + lane) * 16u,
                          (u32x4){pk(v0[0], v0[1]), pk(v0[2], v0[3]), pk(v1_[0], v1_[1]), pk(v1_[2], v1_[3])});
                }
            }
            publish(p.ws, PH_A2, 5 * fj + cg, ep);
        } else {
            const rsrc_t rb2 = mkrs(p.b2, D * 2);
            f32x4 a0 = z, a1 = z;
#pragma unroll
            for (int kk = 0; kk < 20; ++kk) {
                const u32x4 bf = ld16(rb2, (128 * rot5(cg, kk >> 2) + 32 * (kk & 3) + 8 * q) * 2u);
                a0 = mfma(bf, Q[0][kk], a0); a1 = mfma(bf, Q[1][kk], a1);
            }
            const float am = (cg == 0 && w == 0 && n < 2) ? AMP0 : AMP;
            cst4(rws, q < 2 ? O_B2P + (128 * cg + 32 * w + 16 * q + n) * 4u : 0xfffffff0u, __float_as_uint(q == 0 ? a0[0] * am : a1[0] * AMP));
            ep = rfl64(ep_in) + 1;
            publish(p.ws, PH_A2, 80 + cg, ep);
        }
    } else ep = rfl64(ep_in) + 1;

    const int u = ar >> 1, half = ar & 1;
    {
        const rsrc_t rw2 = mkrs(p.w2, D * F * 2);
#pragma unroll
        for (int i = 0; i < 5; ++i) {
            const unsigned o = (w * 5 + i) * 1024u + lane * 16u, row = o / 640u;
            dma16(rw2, (32 * u + row) * 1280u + half * 640u + (o - row * 640u), 0u, lb + LDS_SCR + (w * 5 + i) * 1024u);
        }
    }
    wait_flags(p.ws, PH_A1, w1f_idx(m, lane), ep);

    w1f_dma<0, 30>(w1f_adr(rws, lb, m, w, lane));
    VMBAR(30);
    fa = peek_flag(p.ws, PH_A2, a2_idx(ct, w, lane));
#pragma unroll
    for (int i = 0; i < 5; ++i) {
        const int tl = 5 * w + i;
        const char* ap = smem + LDS_SCR + (4 * q + ((lane >> 2) & 3)) * 640 + tl * 32 + (lane & 3) * 8;
        const u32x2 lo = trd(ap), hi = trd(ap + 16 * 640);
        cst16(rws, O_W2T + (((20 * half + tl) * 48 + u) * 64 + lane) * 16u, (u32x4){lo[0], lo[1], hi[0], hi[1]});
    }
    if (PZ2 && MKP == 0) mark_g2(rws, rb, ct, w, lane);
    if (PZH) mark_h(rws, O_HF1, tt, m, w, lane);
    publish(p.ws, PH_A3, ar, ep);
}

template <class H>
DI void gen_qt(u32x4 (&QT)[5][5], int ct, int w, int lane, H hook) {
    const int n = lane & 15, q = lane >> 4;
#pragma unroll
    for (int mt = 0; mt < 5; ++mt) {
        hook(mt);
        const float df = (float)(80 * ct + 16 * mt + n);
        const float t = (float)(80 * w + 2 * q) * df;
        const float k = floorf(t * (1.f / 640.f));
        const float r0 = __builtin_fmaf(-k, 640.f, t) * (1.f / 640.f);
        const float d1 = df * (1.f / 640.f), d8 = fract(df * (1.f / 80.f));
        const float c2 = 2.f * vcos(d8);
        f32x2 a[2], b[2];
#pragma unroll
        for (int pr = 0; pr < 2; ++pr) {
            const float ra = pr ? r0 + d1 : r0, rb = ra + d8;
            a[pr] = (f32x2){vcos(ra), vcos(ra + 0.25f)} * AMP;
            b[pr] = (f32x2){vcos(rb), vcos(rb + 0.25f)} * AMP;
        }
#pragma unroll
        for (int s = 0; s < 10; ++s) {
#pragma unroll
            for (int pr = 0; pr < 2; ++pr) {
                f32x2 v;
                if (s == 0) v = a[pr];
                else if (s == 1) v = b[pr];
                else { v = __builtin_elementwise_fma((f32x2){c2, c2}, b[pr], -a[pr]); a[pr] = b[pr]; b[pr] = v; }
                QT[mt][s >> 1][2 * (s & 1) + pr] = pk(v[0], v[1]);
            }
        }
        if (w == 0 && q == 0) QT[mt][0][0] = pk(AMP0, (n & 1) ? -AMP0 : AMP0);
#pragma unroll
        for (int kk = 0; kk < 5; ++kk) PIN(QT[mt][kk]);
        SB();
    }
}

#define A2P 0
#define A1W 2
#define A1P 1
#define D1N 0
#define DTN 0
#define D3N 3
#define D4N 2
#define DDN 8
constexpr int DB1 = 4 * D1N, DB3 = DB1 + 4 * DTN, DB4 = DB3 + 4 * D3N, DB6 = imin(30, DB4 + 4 * D4N), DB7 = imin(30, DB6 + DDN);
#define FASTC 1
#define QMID -1
#define CMID -1
template <int L, class H>
DI void phase_c(const Params& p, int g, int tt, int m1, int rb, int ct, u64 ep, u64& fa, char* lds0, char* smem, H mid) {
    constexpr unsigned o_in = L ? O_XH2 : O_XH0, o_st = L ? O_ST2 : O_ST0, o_out = L ? O_XH3 : O_XH1;
    constexpr int SO = L ? 28 : 8;
    IDS;
    const rsrc_t rws = mkrs(p.ws, WS_USED);
    const int c = w >> 1, kh = w & 1;
    u32x4 B1r[2], B1i[2], A3[4][4], A3n[4][4], A6[2][2];
    f32x2 twc[4][2], tws[4][2];
    u64 fv;
    constexpr bool MK = L && PZ2;
    u32x4 xin[4], st[L ? 8 : 2], wf[4];
    auto ldw = [&]() {
#pragma unroll
        for (int mt = 0; mt < 4; ++mt) wf[mt] = cld16(rws, O_WFQ + ((2 * g + c) * 512 + (kh * 4 + mt) * 64 + lane) * 16u);
    };
    auto ldx = [&]() {
        const int h = q & 1, hi = q >> 1;
        if constexpr (L) {
#pragma unroll
            for (int cp = 0; cp < 8; ++cp) st[cp] = cld16(rws, o_st + ((cp * 4 + w) * 64 + lane) * 16u);
        } else {
#pragma unroll
            for (int hf = 0; hf < 2; ++hf) st[hf] = cld16(rws, o_st + ((w * 64 + lane) * 2 + hf) * 16u);
        }
#pragma unroll
        for (int ii = 0; ii < 4; ++ii) xin[ii] = cld16(rws, o_in + g * GS + (2 * (8 * w + 4 * hi + ii) + h) * 256u + n * 16u);
    };
    {
        if (CMID < 0) { mid(); SB(); }
        const float ka = (float)(16 * kh + n);
        const unsigned sg1 = (n & 1) ? 0x80008000u : 0u;
#pragma unroll
        for (int m = 0; m < 4; ++m) {
            const float r = (float)(4 * q + m) * (1.f / 32.f) * ka;
            const float cs = vcos(r), sn = vsin(r);
            B1r[0][m] = pk(cs, sn); B1i[0][m] = pk(-sn, cs);
            B1r[1][m] = B1r[0][m] ^ sg1; B1i[1][m] = B1i[0][m] ^ sg1;
        }
        PIN(B1r[0]); PIN(B1r[1]); PIN(B1i[0]); PIN(B1i[1]);
        const float rc = vcos(ka * (1.f / 128.f)), rsn = vsin(ka * (1.f / 128.f));
        sfor<5>([&](auto bc) {
            constexpr int bt = bc;
            if constexpr (CMID == bt) { SB(); mid(); SB(); }
            if constexpr (bt < 4) {
                if constexpr (FASTC && bt > 0) {
#pragma unroll
                    for (int r2 = 0; r2 < 2; ++r2) {
                        twc[bt][r2] = __builtin_elementwise_fma(twc[bt - 1][r2], (f32x2){rc, rc}, -(tws[bt - 1][r2] * rsn));
                        tws[bt][r2] = __builtin_elementwise_fma(tws[bt - 1][r2], (f32x2){rc, rc}, twc[bt - 1][r2] * rsn);
                    }
                } else {
#pragma unroll
                    for (int r = 0; r < 4; ++r) {
                        const float a = (float)(16 * bt + 4 * q + r) * ka * (1.f / 2048.f);
                        twc[bt][r >> 1][r & 1] = vcos(a); tws[bt][r >> 1][r & 1] = vsin(a);
                    }
                }
                PIN(twc[bt][0]); PIN(twc[bt][1]); PIN(tws[bt][0]); PIN(tws[bt][1]);
            }
        });
        SB();
        if constexpr (!MK) fv = peek_flag(p.ws, L ? PH_G2 : PH_P0, L || tid < 160 ? tid : 0);
        else { ldw(); if (PZP == 0) ldx(); }
        SB();
        {
            const float qc = (n & 1) ? 0.f : (n & 2) ? -1.f : 1.f, qs = (n & 1) ? ((n & 2) ? -1.f : 1.f) : 0.f;
#pragma unroll
            for (int m = 0; m < 4; ++m) {
                const float a = (float)n * (float)(4 * q + m) * (1.f / 64.f);
                f32x2 v = {vcos(a), vsin(a)};
#pragma unroll
                for (int bt = 0; bt < 4; ++bt) {
                    if (bt) v = __builtin_elementwise_fma(v, (f32x2){qc, qc}, (f32x2){-v[1], v[0]} * qs);
                    const float cs = v[0], sn = v[1];
#pragma unroll
                    for (int mt = 0; mt < 4; ++mt) {
                        const int k4 = (mt * m) & 3;
                        A3[mt][bt][m] = k4 == 0 ? pk(cs, sn) : k4 == 1 ? pk(-sn, cs) : k4 == 2 ? pk(-cs, -sn) : pk(sn, -cs);
                        if (A3N == 1 || A3N == 2) A3n[mt][bt][m] = k4 == 3 ? pk(cs, sn) : k4 == 0 ? pk(-sn, cs) : k4 == 1 ? pk(-cs, -sn) : pk(sn, -cs);
                    }
                }
            }
        }
        if constexpr (MK && PZP == 1) {
            PIN(A3[0][0]); PIN(A3[1][1]); PIN(A3[2][2]); PIN(A3[3][3]);
            SB(); ldx(); SB();
        }
        const float rio = (float)(n & 1) * -0.25f;
        {
            const unsigned sg6 = (n & 2) ? 0x80008000u : 0u;
#pragma unroll
            for (int m = 0; m < 4; ++m) {
                const float a = (float)(16 * (w >> 1) + (n >> 1)) * (float)(4 * q + m) * (1.f / 32.f) + rio;
                const float x = vcos(a) * (1.f / 2048.f), y = vcos(a + 0.25f) * (1.f / 2048.f);
                A6[0][0][m] = pk(x, y);
                A6[1][0][m] = m == 0 ? pk(x, y) : m == 1 ? pk(y, -x) : m == 2 ? pk(-x, -y) : pk(-y, x);
                A6[0][1][m] = A6[0][0][m] ^ sg6; A6[1][1][m] = A6[1][0][m] ^ sg6;
            }
        }
#pragma unroll
        for (int i = 0; i < 4; ++i) {
            if (i < 2) { PIN(B1r[i]); PIN(B1i[i]); PIN(A6[i][0]); PIN(A6[i][1]); }
            PIN(A3[i][0]); PIN(A3[i][1]); PIN(A3[i][2]); PIN(A3[i][3]);
            if (A3N == 1 || A3N == 2) { PINA(A3n[i][0]); PINA(A3n[i][1]); PINA(A3n[i][2]); PINA(A3n[i][3]); }
            PIN(twc[i][0]); PIN(twc[i][1]); PIN(tws[i][0]); PIN(tws[i][1]);
        }
    }
    SB();

    auto turn = [&]() {
        if (A3N == 3) {
            SB();
#pragma unroll
            for (int i = 0; i < 64; ++i) {
                unsigned d;
                asm volatile("v_pk_add_f16 %0, %1, 0 op_sel:[1,0] op_sel_hi:[0,0] neg_lo:[1,0]" : "=v"(d) : "v"(A3[i >> 4][(i >> 2) & 3][i & 3]));
                A3n[i >> 4][(i >> 2) & 3][i & 3] = d;
            }
            SB();
        }
    };
    if constexpr (MK) {
        if (PZP == 2) ldx();
        turn();
        int s = 0;
#pragma unroll 1
        for (; s < 400000; ++s) {
            unsigned m = 0;
#pragma unroll
            for (int cp = 0; cp < 8; ++cp) m = umax4(m, st[cp]);
#pragma unroll
            for (int ii = 0; ii < 4; ++ii) m = umax4(m, xin[ii]);
            if (!__any(m == MARK)) break;
            __builtin_amdgcn_s_sleep(1);
            ldx();
        }

        bar();
    } else {
        turn();
        finish_wait(p.ws, L ? PH_G2 : PH_P0, L || tid < 160 ? tid : 0, ep, fv);
        bar();
        ldw(); ldx();
        if (!L && A1P == 0) fv = peek_flag(p.ws, PH_A1, w1f_idx(m1, lane));
    }
    SB();
    u32x4* FR = (u32x4*)smem;
    u32x4* RAW = (u32x4*)(smem + 16384);
    {
        const int h = q & 1, hi = q >> 1;
        float rs[4][2];
        const unsigned sel0 = (unsigned)fresh_s(0x00003f80), sel1 = (unsigned)fresh_s(0x3f800000);
#pragma unroll
        for (int ii = 0; ii < 4; ++ii)
#pragma unroll
            for (int hf = 0; hf < 2; ++hf) {
                if constexpr (L) {
                    float s = 0.f;
#pragma unroll
                    for (int cp = 0; cp < 8; ++cp) s = dot2(st[cp][ii], hf ? sel1 : sel0, s);
                    rs[ii][hf] = __builtin_amdgcn_rsqf(__builtin_fmaf(s, 1.f / 640.f, 1e-5f));
                } else rs[ii][hf] = __uint_as_float(st[hf][ii]);
            }
#pragma unroll
        for (int ii = 0; ii < 4; ++ii) RAW[((8 * w + 4 * hi + ii) * 2 + h) * 16 + n] = xin[ii];
        const int aq = 2 * w + hi;
#pragma unroll
        for (int hf = 0; hf < 2; ++hf)
#pragma unroll
            for (int cc = 0; cc < 2; ++cc) {
                u32x4 o;
#pragma unroll
                for (int ii = 0; ii < 4; ++ii) {
                    const unsigned u = xin[ii][hf * 2 + cc];
                    float re = blo(u) * rs[ii][hf];
                    if (cc == 0 && g == 0) re = 0.f;
                    o[ii] = pk(re, bhi(u) * rs[ii][hf]);
                }
                FR[((cc * 4 + 2 * h + hf) * 2 + (aq >> 2)) * 64 + (aq & 3) * 16 + n] = o;
            }
    }
    bar();

    const W1Adr wa = w1f_adr(rws, (unsigned)(size_t)(LAS char*)lds0, m1, w, lane);
    u64 fa1 = fv;
    if (!L && A1P == 1) { fa1 = peek_flag(p.ws, PH_A1, w1f_idx(m1, lane)); SB(); }
    if (!L && A1W == 0) finish_wait(p.ws, PH_A1, w1f_idx(m1, lane), ep, fa1);
    if (!L && PZ2 && MKP == 0 && MKC == 0) mark_g2(rws, rb, ct, w, lane);
    if (PZH) mark_h(rws, L ? O_HF2 : O_HF1, tt, m1, w, lane);
    f32x4 tr[4], ti[4];
    {
        u32x4 a[4][2];
#pragma unroll
        for (int bt = 0; bt < 4; ++bt) { a[bt][0] = FR[((c * 4 + bt) * 2 + 0) * 64 + lane]; a[bt][1] = FR[((c * 4 + bt) * 2 + 1) * 64 + lane]; }
        SB();
        sfor<4>([&](auto bc) {
            constexpr int bt = bc;
            const f32x4 z = {0.f, 0.f, 0.f, 0.f};
            tr[bt] = mfma(a[bt][1], B1r[1], mfma(a[bt][0], B1r[0], z));
            ti[bt] = mfma(a[bt][1], B1i[1], mfma(a[bt][0], B1i[0], z));
            if constexpr (!L) { w1f_dma<D1N * bt, D1N * (bt + 1)>(wa); if (PZ2 && MKC == 2) mark_g2(rws, rb, ct, w, lane, bt); SB(); }
            if constexpr (((PH2 >> L) & 1) && !MKH) { mark_h2<160>(rws, L ? O_HF2 : O_HF1, g, w, lane, bt); SB(); }
        });
    }
    bar();
    if (!L && A1P == 2) { fa1 = peek_flag(p.ws, PH_A1, w1f_idx(m1, lane)); SB(); }
    if (!L && A1W == 1) { finish_wait(p.ws, PH_A1, w1f_idx(m1, lane), ep, fa1); SB(); }
    u32x4 P[4], Pn[4];
    sfor<4>([&](auto bc) {
        constexpr int bt = bc;
#pragma unroll
        for (int r2 = 0; r2 < 2; ++r2) {
            const f32x2 a = {tr[bt][2 * r2], tr[bt][2 * r2 + 1]}, b = {ti[bt][2 * r2], ti[bt][2 * r2 + 1]};
            const f32x2 xr = a * twc[bt][r2] + b * tws[bt][r2], xi = b * twc[bt][r2] - a * tws[bt][r2];
            P[bt][2 * r2] = pk(xr[0], xi[0]); P[bt][2 * r2 + 1] = pk(xr[1], xi[1]);
            if (!A3N) { Pn[bt][2 * r2] = pk(xi[0], -xr[0]); Pn[bt][2 * r2 + 1] = pk(xi[1], -xr[1]); }
        }
        if constexpr (!L) { w1f_dma<DB1 + DTN * bt, DB1 + DTN * (bt + 1)>(wa); SB(); }
    });
    if (!L && A1P == 3) { fa1 = peek_flag(p.ws, PH_A1, w1f_idx(m1, lane)); SB(); }
    if (!L && A1W == 2) { finish_wait(p.ws, PH_A1, w1f_idx(m1, lane), ep, fa1); SB(); }
    u32x4 R[4], Rn[4];
    sfor<4>([&](auto mc) {
        constexpr int mt = mc;
        f32x4 zr = {0.f, 0.f, 0.f, 0.f}, zi = {0.f, 0.f, 0.f, 0.f};
#pragma unroll
        for (int bt = 0; bt < 4; ++bt) { zr = mfma(A3[mt][bt], P[bt], zr); zi = A3N ? mfma(A3n[mt][bt], P[bt], zi) : mfma(A3[mt][bt], Pn[bt], zi); }
        if constexpr (!L) { w1f_dma<DB3 + D3N * mt, DB3 + D3N * (mt + 1)>(wa); if (PZ2 && MKC == 3) mark_g2(rws, rb, ct, w, lane, mt); SB(); }
#pragma unroll
        for (int r2 = 0; r2 < 2; ++r2) {
            const unsigned f0 = wf[mt][2 * r2], f1 = wf[mt][2 * r2 + 1];
            const f32x2 fr = {blo(f0), blo(f1)}, fi = {bhi(f0), bhi(f1)}, a = {zr[2 * r2], zr[2 * r2 + 1]}, b = {zi[2 * r2], zi[2 * r2 + 1]};
            const f32x2 yr = __builtin_elementwise_fma(a, fr, -(b * fi)), ny = __builtin_elementwise_fma(-a, fi, -(b * fr));
            R[mt][2 * r2] = pk(yr[0], ny[0]); R[mt][2 * r2 + 1] = pk(yr[1], ny[1]);
        }
    });

    SB();
    if (!L && A1W == 3) { finish_wait(p.ws, PH_A1, w1f_idx(m1, lane), ep, fa1); SB(); }
    unsigned* TR = (unsigned*)smem;
    sfor<4>([&](auto bc) {
        constexpr int bt = bc;
        f32x4 ur = {0.f, 0.f, 0.f, 0.f}, ui = {0.f, 0.f, 0.f, 0.f};
#pragma unroll
        for (int kt = 0; kt < 4; ++kt) { ur = mfma(A3[bt][kt], R[kt], ur); ui = A3N ? mfma(A3n[bt][kt], R[kt], ui) : mfma(A3[bt][kt], Rn[kt], ui); }
        if constexpr (!L) { w1f_dma<imin(30, DB4 + D4N * bt), imin(30, DB4 + D4N * (bt + 1))>(wa); if (PZ2 && MKC == 4) mark_g2(rws, rb, ct, w, lane, bt); SB(); }
#pragma unroll
        for (int r2 = 0; r2 < 2; ++r2) {
            const f32x2 a = {ur[2 * r2], ur[2 * r2 + 1]}, b = {ui[2 * r2], ui[2 * r2 + 1]};
            const f32x2 xr = A3N ? a * twc[bt][r2] + b * tws[bt][r2] : a * twc[bt][r2] - b * tws[bt][r2];
            const f32x2 xi = A3N ? a * tws[bt][r2] - b * twc[bt][r2] : b * twc[bt][r2] + a * tws[bt][r2];
#pragma unroll
            for (int e = 0; e < 2; ++e) {
                const int r = 2 * r2 + e;
                const int b_ = 16 * bt + 4 * q + r;
                const int kq = 4 * kh + (n >> 2), f = (2 * q + r2) & 7;
                TR[c * 2048 + b_ * 32 + ((kq ^ f) << 2) + (n & 3)] = pk(xr[e], xi[e]);
            }
        }
        SB();
    });
    bar();

    if (!L && !A2P) fa = peek_flag(p.ws, PH_A2, a2_idx(ct, w, lane));
    {
        const int h = w & 1, ah = w >> 1;
        const u32x4* TRv = (const u32x4*)smem;
        u32x4 bq[2][2][2], raw[2][2];
#pragma unroll
        for (int hf = 0; hf < 2; ++hf)
#pragma unroll
            for (int cc = 0; cc < 2; ++cc) {
                const int b_ = 16 * (2 * h + hf) + n, f = (n >> 1) & 7;
                bq[hf][cc][0] = TRv[cc * 512 + b_ * 8 + ((0 + q) ^ f)]; bq[hf][cc][1] = TRv[cc * 512 + b_ * 8 + ((4 + q) ^ f)];
            }
#pragma unroll
        for (int rt = 0; rt < 2; ++rt)
#pragma unroll
            for (int e = 0; e < 2; ++e) raw[rt][e] = RAW[((16 * ah + 8 * rt + 2 * q + e) * 2 + h) * 16 + n];
        SB();
        u32x4 o[2][2];
#pragma unroll
        for (int rt = 0; rt < 2; ++rt) {
#pragma unroll
            for (int hf = 0; hf < 2; ++hf)
#pragma unroll
                for (int cc = 0; cc < 2; ++cc) {
                    const unsigned r0 = raw[rt][0][hf * 2 + cc], r1 = raw[rt][1][hf * 2 + cc];
                    f32x4 y = {0.f, 0.f, 0.f, 0.f};
                    y = mfma(A6[rt][0], bq[hf][cc][0], y); y = mfma(A6[rt][1], bq[hf][cc][1], y);
                    if (cc == 0 && g == 0) { y[0] = 0.f; y[2] = 0.f; }
                    o[rt][0][hf * 2 + cc] = pk(y[0] + blo(r0), y[1] + bhi(r0));
                    o[rt][1][hf * 2 + cc] = pk(y[2] + blo(r1), y[3] + bhi(r1));
                }
#pragma unroll
            for (int e = 0; e < 2; ++e)
                cst16(rws, o_out + g * GS + (2 * (16 * ah + 8 * rt + 2 * q + e) + h) * 256u + n * 16u, o[rt][e]);
        }
    }

    if constexpr (!L) {
        SB();
        w1f_dma<DB6, DB7>(wa);
        if (A2P) { SB(); fa = peek_flag(p.ws, PH_A2, a2_idx(ct, w, lane)); SB(); }
        publish<DB7 - DB6 + (A2P ? 1 : 0)>(p.ws, PH_C1, g, ep);

        w1f_dma<DB7, 30>(wa);
        SB();
    } else {
        if (QMID < 0) publish(p.ws, PH_C2, g, ep);

    }
}

#define WE0 8
#define WM0 40
#define WG0 12
#define WE1 3
#define WM1 25
#define WG1 12
#define XLD2(r, v, so) __builtin_amdgcn_raw_buffer_load_b128(r, v, so, 16)
#define A2O 0
#define DPV 6
#define GST 0
#define RSI 2
#define ACV 0
#define PKS 8
#define PUBS 4
#define WADR 1
struct WAdr { unsigned v[5], s[3]; };
DI WAdr wadr(unsigned wo) {
    WAdr a;
#pragma unroll
    for (int m = 0; m < 5; ++m) { a.v[m] = wo + m * 49152u; if (WADR) PIN(a.v[m]); }
    a.s[0] = 0; a.s[1] = (unsigned)fresh_s(4096); a.s[2] = (unsigned)fresh_s(8192);
    return a;
}
constexpr int it_k(int u) { return PH2 ? (u < 4 ? 3 * u + 2 : 3 * ((u - 4) >> 1) + (u & 1)) : PZH ? 3 * (u & 3) + (u >> 2) : u; }
#define WLD(i) W[(i) % 5][it_k((i) / 5)] = __builtin_amdgcn_raw_buffer_load_b128(rws, wa.v[(i) % 5] + (it_k((i) / 5) & 3) * 1024u, wa.s[it_k((i) / 5) >> 2], 16)
template <int L>
DI void phase_g1(const Params& p, int g, int tt, int fs, int rb, int ct, u32x4 (&W)[5][12], unsigned wo, u32x4 (&xg)[2][5][2], u32x4 (&QT)[5][5], u64 ep, u64 fa, char* smem) {
    constexpr unsigned o_in = L ? O_XH3 : O_XH1, o_hf = L ? O_HF2 : O_HF1;
    constexpr int SO = L ? 36 : 17;
    constexpr int R = DPV;
    IDS;
    const rsrc_t rws = mkrs(p.ws, WS_USED), rb1 = mkrs(p.b1, F * 2);
    const WAdr wa = wadr(wo);
    u32x2 bb[6];
    constexpr int WE = L ? WE1 : WE0, WM = L ? WM1 : WM0, WG = L ? WG1 : WG0;
    u64 fv;
    const int fi = lane < 40 ? (L ? PH_C2 : PH_C1) * 256 + w * 40 + lane
                             : ((PH2 >> L) & 1) && MKH ? (L ? PH_G2 * 256 + w * 24 + lane - 40 : PH_A2 * 256 + w * 20 + (lane - 40) % 20)
                             : PZH ? (L ? PH_G2 : PH_A3) * 256 + w * 24 + lane - 40 : (L ? PH_C2 : PH_C1) * 256 + w * 40;
    auto first = [&]() {
#pragma unroll
        for (int mt = 0; mt < 6; ++mt) bb[mt] = ld8(rb1, (96 * fs + 16 * mt + 4 * q) * 2u);
        if (!L && !A2O) finish_wait(p.ws, PH_A2, a2_idx(ct, w, lane), ep, fa);
        fv = peek_flag(p.ws, 0, fi);
        if (!L && A2O) { SB(); finish_wait(p.ws, PH_A2, a2_idx(ct, w, lane), ep, fa); }
        SB();
    };
    if constexpr (L) {
        constexpr int Q0 = QMID < 0 ? 0 : QMID;
        gen_qt(QT, ct, w, lane, [&](int mt) {
            if (mt == Q0) {
                if (QMID >= 0 && g >= 0) publish(p.ws, PH_C2, g, ep);
                first();
            }
            if (mt >= Q0) {
#pragma unroll
                for (int i = (mt - Q0) * WE / (5 - Q0); i < (mt + 1 - Q0) * WE / (5 - Q0); ++i) WLD(i);
            }
        });
    } else {
        if (PZ2 && MKP == 1) mark_g2(rws, rb, ct, w, lane);
        first();
#pragma unroll
        for (int i = 0; i < WE; ++i) WLD(i);
        if (PZ2 && MKP == 2) mark_g2(rws, rb, ct, w, lane);
    }
    SB();
    finish_wait(p.ws, 0, fi, ep, fv);
    bar();
    const int blk = 4 * tt + w;
    unsigned xv[4], gv[2], sj[10];
    xv[0] = o_in + q * GS + blk * 256u + n * 16u;
#pragma unroll
    for (int a = 1; a < 4; ++a) { xv[a] = xv[0] + 40 * a * GS; PIN(xv[a]); }
    gv[0] = O_XH3 + (40 * w + q) * GS + (2 * rb + (w >> 1)) * 256u + n * 16u;
    gv[1] = gv[0] ^ 256u;
    sj[0] = 0;
#pragma unroll
    for (int b = 1; b < 10; ++b) sj[b] = (unsigned)fresh_s(4 * b * GS);
    auto xld = [&](int j) -> u32x4 { return XLD2(rws, xv[j / 10], sj[j % 10]); };
    u32x4 l[R][2];
#pragma unroll
    for (int kk = 0; kk < R - 1; ++kk) { l[kk][0] = xld(2 * kk); l[kk][1] = xld(2 * kk + 1); }
    SB();
    const u32x4* WL0 = (const u32x4*)smem + lane;
    unsigned wl1 = (unsigned)(size_t)(LAS char*)smem + 61440u + lane * 16u;
    PIN(wl1);
    auto ard = [&](int mt, int kk) -> u32x4 { return mt < 3 ? WL0[(mt * 20 + kk) * 64] : *(const LAS u32x4*)(size_t)(wl1 + ((mt - 3) * 20 + kk) * 1024u); };
    f32x4 acc[6][2], gr0 = {0.f, 0.f, 0.f, 0.f}, gr1 = {0.f, 0.f, 0.f, 0.f};
    float ss[2] = {0.f, 0.f};
    float rs[2], rg[2][4], rt[2];
    unsigned ra[2][2];
    auto rsop = [&](int hf, int o) {
        if (o < 4) { rg[hf][o] = hf ? gr1[o] : gr0[o]; PIN(rg[hf][o]); }
        else if (o < 8) { rt[hf] = __builtin_amdgcn_inverse_ballot_w64(0x0000010000100001ull << (o - 4) | 1ull << (56 + o)) ? rg[hf][o - 4] : o == 4 ? 0.f : rt[hf]; PIN(rt[hf]); }
        else if (o == 8 || o == 10) {
            auto a = o == 8 ? __builtin_amdgcn_permlane16_swap(__float_as_uint(rt[hf]), __float_as_uint(rt[hf]), false, false)
                            : __builtin_amdgcn_permlane32_swap(__float_as_uint(rt[hf]), __float_as_uint(rt[hf]), false, false);
            ra[hf][0] = a[0]; ra[hf][1] = a[1]; PIN(ra[hf][0]); PIN(ra[hf][1]);
        }
        else if (o == 9 || o == 11) { rt[hf] = __uint_as_float(ra[hf][0]) + __uint_as_float(ra[hf][1]); PIN(rt[hf]); }
        else if (o == 12) { rt[hf] = __builtin_fmaf(rt[hf], 1.f / 640.f, 1e-5f); PIN(rt[hf]); }
        else { rs[hf] = __builtin_amdgcn_rsqf(rt[hf]); PIN(rs[hf]); }
    };
#pragma unroll
    for (int mt = 0; mt < 6; ++mt) { acc[mt][0] = (f32x4){0.f, 0.f, 0.f, 0.f}; acc[mt][1] = (f32x4){0.f, 0.f, 0.f, 0.f}; }
    u32x4 A[2][6];
#pragma unroll
    for (int mt = 0; mt < 6; ++mt) A[0][mt] = ard(mt, 0);
    swp(l[0][0], 2, l[0][1], 0); swp(l[0][0], 3, l[0][1], 1);
    if (q == 0) { l[0][0][0] &= 0xffff0000u; l[0][1][0] &= 0xffff0000u; }
    SB();
#pragma unroll
    for (int kk = 0; kk < 20; ++kk) {
        const int c = kk & 1, s = kk % R, sn = (kk + 1) % R, sf = (kk + R - 1) % R;
#pragma unroll
        for (int i = 0; i < 14; ++i) {
            const bool last = RSI && kk == 19;
            if (i == (last ? 0 : 2)) gr0 = mfma(l[s][0], l[s][0], gr0);
            else if (i == (last ? 1 : 5)) gr1 = mfma(l[s][1], l[s][1], gr1);
            else {
                const int ix = last ? i - 2 : i < 2 ? i : i < 5 ? i - 1 : i - 2;
                acc[ix >> 1][ix & 1] = mfma(A[c][ix >> 1], l[s][ix & 1], acc[ix >> 1][ix & 1]);
            }
            if (last && i >= 4) {
#pragma unroll
                for (int o = 3 * (i - 4); o < 3 * (i - 3) && o < 28; ++o) rsop(o & 1, o >> 1);
                if (RSI == 2 && i >= 5) PIN(acc[(i - 5) >> 1][(i - 5) & 1]);
            }
            if (i < 6 && kk < 19) A[c ^ 1][i] = ard(i, kk + 1);
            if ((i == 6 || i == 7) && kk + R - 1 < 20) l[sf][i - 6] = xld(2 * (kk + R - 1) + i - 6);
            if (i == 8 && kk < 19) swp(l[sn][0], 2, l[sn][1], 0);
            if (i == 9 && kk < 19) swp(l[sn][0], 3, l[sn][1], 1);
            if (L && i == 10) xg[kk / 10][(kk % 10) >> 1][kk & 1] = __builtin_amdgcn_raw_buffer_load_b128(rws, gv[kk / 10], sj[kk % 10], 16);
            if (i == 11 || i == 12) {
                const int t = 2 * kk + i - 11;
#pragma unroll
                for (int x = WE + t * WM / 40; x < WE + (t + 1) * WM / 40; ++x) WLD(x);
            }
            if (i == 13 && kk < 19) __builtin_amdgcn_s_waitcnt(0xC07F);
            if (ACV && i == 13) {
#pragma unroll
                for (int x = 0; x < 12; ++x) PIN(acc[x >> 1][x & 1]);
            }
            SB();
        }

    }

    if (!RSI || GST) {
        const int r = n & 3;
        const float s0 = r == 0 ? gr0[0] : r == 1 ? gr0[1] : r == 2 ? gr0[2] : gr0[3];
        const float s1 = r == 0 ? gr1[0] : r == 1 ? gr1[1] : r == 2 ? gr1[2] : gr1[3];
        const bool dg = q == (n >> 2);
        rs[0] = __builtin_amdgcn_rsqf(__builtin_fmaf(sumq(dg ? s0 : 0.f), 1.f / 640.f, 1e-5f));
        rs[1] = __builtin_amdgcn_rsqf(__builtin_fmaf(sumq(dg ? s1 : 0.f), 1.f / 640.f, 1e-5f));
    }
    constexpr float C0 = -2.3022081985f, C1 = -0.1029432397f;
    constexpr bool HM = (PH2 >> L) & 1;
    u32x4 oh;
    SB();
#pragma unroll
    for (int u = 0; u < 3; ++u) {
        f32x2 bv[4];
#pragma unroll
        for (int i = 0; i < 4; ++i) bv[i] = (f32x2){blo(bb[2 * u + (i >> 1)][i & 1]), bhi(bb[2 * u + (i >> 1)][i & 1])};
#pragma unroll
        for (int hf = 0; hf < 2; ++hf) {
            const int gi = 2 * (2 * u + hf);
            f32x2 x[4], t[4];
#pragma unroll
            for (int i = 0; i < 4; ++i) {
                const f32x4 a = acc[2 * u + (i >> 1)][hf];
                x[i] = __builtin_elementwise_fma((f32x2){a[2 * (i & 1)], a[2 * (i & 1) + 1]}, (f32x2){rs[hf], rs[hf]}, bv[i]);
            }
#pragma unroll
            for (int i = 0; i < 4; ++i) t[i] = x[i] * x[i];
#pragma unroll
            for (int i = 0; i < 4; ++i) t[i] = __builtin_elementwise_fma(t[i], (f32x2){C1, C1}, (f32x2){C0, C0});
#pragma unroll
            for (int i = 0; i < 4; ++i) t[i] = x[i] * t[i];
            SB();
#pragma unroll
            for (int i = WE + WM + gi * WG / 12; i < WE + WM + (gi + 1) * WG / 12; ++i) WLD(i);
#pragma unroll
            for (int i = 0; i < 4; ++i) t[i] = (f32x2){__builtin_amdgcn_exp2f(t[i][0]), __builtin_amdgcn_exp2f(t[i][1])};
            SB();
#pragma unroll
            for (int i = 0; i < 4; ++i) t[i] = t[i] + 1.f;
            SB();
#pragma unroll
            for (int i = WE + WM + (gi + 1) * WG / 12; i < WE + WM + (gi + 2) * WG / 12; ++i) WLD(i);
#pragma unroll
            for (int i = 0; i < 4; ++i) t[i] = (f32x2){__builtin_amdgcn_rcpf(t[i][0]), __builtin_amdgcn_rcpf(t[i][1])};
            SB();
            u32x4 o;
#pragma unroll
            for (int i = 0; i < 4; ++i) x[i] = x[i] * t[i];
#pragma unroll
            for (int i = 0; i < 4; ++i) o[i] = PZH || (HM && u == 2) ? unmark(pk(x[i][0], x[i][1])) : pk(x[i][0], x[i][1]);
            if (HM && u == 2 && hf == 0) oh = o;
            else {
                if (HM && u == 2) {
                    asm volatile("s_waitcnt vmcnt(%0)" :: "n"(WG - 8 * WG / 12) : "memory");
                    cst16(rws, o_hf + (((2 * blk) * 48 + 3 * fs + u) * 64 + lane) * 16u, oh);
                }
                cst16(rws, o_hf + (((2 * blk + hf) * 48 + 3 * fs + u) * 64 + lane) * 16u, o);
            }
            SB();
        }
    }

    if (!PZH && !HM && !(L && PUBS > 0)) publish(p.ws, L ? PH_G1B : PH_G1A, tt * 16 + fs, ep);

}

constexpr int it_u(int i) { return i < 32 ? i >> 2 : 8 + (i & 3); }
constexpr int it_j(int i) { return i < 32 ? i & 3 : i < 36 ? 3 : i < 40 ? 1 : i < 44 ? 2 : 0; }
#define HSL 0
#define HDL 0
struct HAdr { unsigned v[4], s[3]; };
DI HAdr hadr(unsigned ho, int w) {
    HAdr a;
#pragma unroll
    for (int j = 0; j < 4; ++j) { a.v[j] = ho + (j ^ w) * 49152u; PIN(a.v[j]); }
    a.s[0] = 0; a.s[1] = (unsigned)fresh_s(4096); a.s[2] = (unsigned)fresh_s(8192);
    return a;
}
DI u32x4 hld(rsrc_t rws, const HAdr& a, int i) {
    const int k = it_k(it_u(i));
    return __builtin_amdgcn_raw_buffer_load_b128(rws, a.v[it_j(i)] + (k & 3) * 1024u, a.s[k >> 2], 16);
}
DI bool marked(u32x4 v) { return __any(umax3(umax3(v[0], v[1], v[2]), v[3], 0u) == MARK); }
DI u32x4 hagain(rsrc_t rws, const HAdr& ho, int i) {
    u32x4 v;
#pragma unroll 1
    for (int s = 0; s < 400000; ++s) {
        asm volatile("" ::: "memory");
        v = hld(rws, ho, i);
        if (!marked(v)) break;
        if (HSL > 0) __builtin_amdgcn_s_sleep(HSL);
    }
    return v;
}
DI void hfirst(rsrc_t rws, const HAdr& ho, u32x4 (&hb)[RN]) {
    unsigned m = 0;
#pragma unroll
    for (int x = 0; x < RN; ++x) m = umax4(m, hb[x]);
    if (__builtin_expect(__any(m == MARK), 0)) {
#pragma unroll 1
        for (int s = 0; s < 400000; ++s) {
            if (HSL > 0) __builtin_amdgcn_s_sleep(HSL);
            asm volatile("" ::: "memory");
#pragma unroll
            for (int x = 0; x < RN; ++x) hb[x] = hld(rws, ho, x);
            m = 0;
#pragma unroll
            for (int x = 0; x < RN; ++x) m = umax4(m, hb[x]);
            if (!__any(m == MARK)) break;
        }
    }
}
template <bool ZERO, class H, class H2>
DI void gemm2(f32x4 (&acc)[4][5], const u32x4 (&W)[5][12], u32x4 (&hb)[RN], rsrc_t rws, const HAdr& ho, int w, int lane, f32x4* sb, f32x4 (&res)[5], const f32x4 (&add)[5], H pre, H2 mid) {
    const f32x4 z = {0.f, 0.f, 0.f, 0.f};
    pre();
    SB();
    unsigned mx = 0;
    u64 bad = 0;
    if (PZH) { if (__builtin_expect(marked(hb[0]), 0)) hb[0] = hagain(rws, ho, 0); SB(); }
    if (PH2 & (ZERO ? 1 : 2)) { hfirst(rws, ho, hb); SB(); }
    f32x4* own = sb + w * 640 + lane;
    f32x4* par = sb + (w ^ 2) * 640 + lane;
    const f32x4* dia = sb + (w ^ 3) * 640 + lane;
    f32x4 in0[5], in1[5], tmp;
    sfor<240>([&](auto gc) {
        constexpr int g = gc, i = g / 5, m = g % 5, u = it_u(i), j = it_j(i), t = g - 180;
        acc[j][m] = mfma(W[m][it_k(u)], hb[i % RN], (ZERO && u == 0) ? z : acc[j][m]);
        if constexpr (m == 4 && i + RN < 48) hb[i % RN] = hld(rws, ho, i + RN);
        if constexpr (PZH && i + 1 < 48) {
            constexpr int nx = (i + 1) % 12;
            if constexpr (m == 0) { mx = umax3(hb[nx][0], hb[nx][1], hb[nx][2]); PIN(mx); }
            if constexpr (m == 1) { mx = umax3(mx, hb[nx][3], 0u); PIN(mx); }
            if constexpr (m == 2) { bad = __builtin_amdgcn_ballot_w64(mx == MARK); asm volatile("" : "+s"(bad)); }
            if constexpr (m == 3) { if (__builtin_expect(bad != 0, 0)) hb[nx] = hagain(rws, ho, i + 1); }
            SB();
        }
        if constexpr (g == MKG) mid(0);
        if constexpr (g == 176) mid(1);
        if constexpr (t >= 1 && t < 6) own[(5 + t - 1) * 64] = acc[3][t - 1];
        if constexpr (t == 12) bar();
        if constexpr (t >= 13 && t < 18) in1[t - 13] = par[(5 + t - 13) * 64];
        if constexpr (t >= 21 && t < 41) {
            constexpr int mm = (t - 21) / 4, ph = (t - 21) % 4;
            if constexpr (ph == 0) { tmp = acc[1][mm]; PIN(tmp); }
            if constexpr (ph == 1) { in1[mm] += tmp; PIN(in1[mm]); }
            if constexpr (ph == 2) par[(5 + mm) * 64] = in1[mm];
        }
        if constexpr (t >= 41 && t < 46) own[(t - 41) * 64] = acc[2][t - 41];
        if constexpr (t == 50) bar();
        if constexpr (t >= 50 && t < 55) { in0[t - 50] = par[(t - 50) * 64]; in1[t - 50] = dia[(5 + t - 50) * 64]; }
        if constexpr (t >= 55) { in0[t - 55] += add[t - 55]; in0[t - 55] += in1[t - 55]; PIN(in0[t - 55]); }
        if constexpr (m == 4 || t >= 0) SB();
    });
#pragma unroll
    for (int m = 0; m < 5; ++m) { tmp = acc[0][m]; PIN(tmp); res[m] = in0[m] + tmp; PIN(res[m]); SB(); }
}

DI void phase_g2(const Params& p, int rb, int ct, u32x4 (&W)[5][12], unsigned wo, u64 ep, char* scr) {
    IDS;
    const rsrc_t rws = mkrs(p.ws, WS_USED);
    if constexpr (WE0 + WM0 + WG0 < 60) {
        const WAdr wa = wadr(wo);
#pragma unroll
        for (int i = WE0 + WM0 + WG0; i < 60; ++i) WLD(i);
        SB();
    }
    const HAdr ho = hadr(O_HF1 + ((4 * rb * 48 + 12 * w) * 64 + lane) * 16u, w);
    u32x4 hb[RN];
    auto ring = [&]() {
#pragma unroll
        for (int i = 0; i < RN; ++i) hb[i] = hld(rws, ho, i);
        SB();
    };
    if (PZH && HDL > 0) __builtin_amdgcn_s_sleep(HDL);
    if (PZH) ring();
    f32x4 acc[4][5], v[5], add[5];
    u32x2 res[5];
#pragma unroll
    for (int mt = 0; mt < 5; ++mt) {
        res[mt] = cld8(rws, O_XH1 + (20 * ct + 4 * mt + q) * GS + (2 * rb + (w >> 1)) * 256u + n * 16u + (w & 1) * 8u);
        add[mt] = __builtin_bit_cast(f32x4, cld16(rws, O_B2P + (80 * ct + 16 * mt + 4 * q) * 4u));
    }
    SB();
    if ((PH2 & 1) && HDL > 0) __builtin_amdgcn_s_sleep(HDL);
    if (!PZH && !(PH2 & 1)) wait_flags(p.ws, 0, lane < 32 ? PH_G1A * 256 + (rb >> 1) * 16 + 4 * w + (lane & 3)
                                  : PH_A3 * 256 + (PZ2 && MKP == 0 ? 24 * w + (lane - 32) % 24 : 2 * (12 * w + (lane - 32) % 12) + (ct >> 2)), ep);
    u64 fm = 0;
    if (!PZH) ring();
    auto sum = [&]() {
#pragma unroll
        for (int mt = 0; mt < 5; ++mt) {
            add[mt] += (f32x4){blo(res[mt][0]), bhi(res[mt][0]), blo(res[mt][1]), bhi(res[mt][1])};
            PIN(add[mt]);
        }
    };
    gemm2<true>(acc, W, hb, rws, ho, w, lane, (f32x4*)scr, v, add, [&]() { if (!PZH) sum(); },
                [&](int x) {
                    if (x == 0) { if (PZH) sum(); if (PZ2 && MKP > 0) fm = peek_flag(p.ws, PH_G1A, tid); }
                    if ((PH2 & 1) && x == 0) fm = peek_flag(p.ws, PH_A3, 24 * w + lane % 24);
                    if ((PH2 & 1) && x == 1) finish_wait(p.ws, PH_A3, 24 * w + lane % 24, ep, fm);
                });

    if (PZ2 && MKP > 0) finish_wait(p.ws, PH_G1A, tid, ep, fm);
    store_xh(v, rws, O_XH2, O_ST2, rb, ct, w, lane, scr);

}

#define XWN (PZH ? 6 : 10)
#define XH0S 6
#define XHN 16
#define XQP 1
#define XHS 10
DI void phase_g4(const Params& p, int pub, int rb, int ct, u32x4 (&W)[5][12], unsigned wo, const u32x4 (&QT)[5][5], const u32x4 (&xg)[2][5][2], u64 ep, char* scr) {
    IDS;
    const rsrc_t rws = mkrs(p.ws, WS_USED), rb2 = mkrs(p.b2, D * 2), rout = mkrs(p.out, S * D * 2);
    const WAdr wa = wadr(wo);
    f32x4 acc[4][5];
    const f32x4 z = {0.f, 0.f, 0.f, 0.f};
    const bool odd = w & 1;
    const int fi = (rb >> 1) * 16 + 4 * w + (lane & 3);
    u64 fv;
    u32x2 bb[5];
    const HAdr ho = hadr(O_HF2 + ((4 * rb * 48 + 12 * w) * 64 + lane) * 16u, w);
    u32x4 hb[RN];
    {
        constexpr int W0 = WE1 + WM1 + WG1;
        unsigned t[8], fa[4], fb[4];
        const u64 om = rfl64(odd ? ~0ull : 0ull);
#pragma unroll
        for (int e = 0; e < 8; ++e) { t[e] = xg[0][0][e >> 2][e & 3]; PIN(t[e]); }
#pragma unroll
        for (int e = 0; e < 4; ++e) { const int x = 4 * (e >> 1) + (e & 1); fa[e] = csel(t[x], t[x + 2], om); }
        asm volatile("s_nop 1");
        SB();
        sfor<100>([&](auto gc) {
            constexpr int g = gc, s = g / 10, i = g % 10, b = s / 5, kk = s % 5, mt = i % 5, o = i / 5, b1 = (s + 1) / 5, k1 = (s + 1) % 5;
            acc[2 * b + o][mt] = mfma(QT[mt][kk], o ? (u32x4){fb[0], fb[1], fb[2], fb[3]} : (u32x4){fa[0], fa[1], fa[2], fa[3]}, kk == 0 ? z : acc[2 * b + o][mt]);
            if constexpr (i < 2) {
#pragma unroll
                for (int e = 2 * i; e < 2 * i + 2; ++e) { const int x = 4 * (e >> 1) + (e & 1); fb[e] = csel(t[x + 2], t[x], om); }
            }
            if constexpr (i >= 2 && i < 6 && s < 9) {
#pragma unroll
                for (int e = 2 * (i - 2); e < 2 * (i - 2) + 2; ++e) { t[e] = xg[b1][k1][e >> 2][e & 3]; PIN(t[e]); }
            }
            if constexpr ((i == 6 || i == 7) && s < 9) {
#pragma unroll
                for (int e = 2 * (i - 6); e < 2 * (i - 6) + 2; ++e) { const int x = 4 * (e >> 1) + (e & 1); fa[e] = csel(t[x], t[x + 2], om); }
            }
            if constexpr (!(PH2 & 2) && i == 8 && s == PKS) fv = peek_flag(p.ws, PH_G1B, fi);
            if constexpr ((PH2 & 2) && s >= XH0S && (i == 3 || i == 4 || i == 9)) {
                constexpr int n0 = XHN * (s - XH0S) / (10 - XH0S), n1 = XHN * (s + 1 - XH0S) / (10 - XH0S), c = i == 3 ? 0 : i == 4 ? 1 : 2;
#pragma unroll
                for (int x = n0 + c * (n1 - n0) / 3; x < n0 + (c + 1) * (n1 - n0) / 3; ++x) hb[x] = hld(rws, ho, x);
            }
            if constexpr (i == 8 && s == 9) {
#pragma unroll
                for (int m = 0; m < 5; ++m) bb[m] = ld8(rb2, (80 * ct + 16 * m + 4 * q) * 2u);
            }
            if constexpr (i >= 8) {
                constexpr int x0 = W0 + (60 - W0) * imin(s, XWN) / XWN, x1 = W0 + (60 - W0) * imin(s + 1, XWN) / XWN, xm = (x0 + x1) / 2;
#pragma unroll
                for (int x = i == 8 ? x0 : xm; x < (i == 8 ? xm : x1); ++x) WLD(x);
            }
            if constexpr (XHS < 10 && g == 10 * XHS - 1) {
                finish_wait(p.ws, PH_G1B, fi, ep, fv);
#pragma unroll
                for (int x = 0; x < 12; ++x) hb[x] = hld(rws, ho, x);
            }
            if constexpr (!(PH2 & 2) && PUBS > 0 && g == 10 * PUBS - 1) {
                SB();
                publish<(60 - W0) * imin(PUBS, XWN) / XWN>(p.ws, PH_G1B, pub, ep);
            }
            SB();
        });
    }
    if (PH2 & 2) {
#pragma unroll
        for (int x = XHN; x < RN; ++x) hb[x] = hld(rws, ho, x);
        SB();
    }

    if (!PZH && !(PH2 & 2) && !(XQP && XHS < 10)) {
        finish_wait(p.ws, PH_G1B, fi, ep, fv);
#pragma unroll
        for (int i = 0; i < RN; ++i) hb[i] = hld(rws, ho, i);
        SB();
    }

    f32x4 v[5], add[5];
    gemm2<false>(acc, W, hb, rws, ho, w, lane, (f32x4*)scr, v, add, [&]() {
#pragma unroll
        for (int mt = 0; mt < 5; ++mt) {
            add[mt] = (f32x4){blo(bb[mt][0]), bhi(bb[mt][0]), blo(bb[mt][1]), bhi(bb[mt][1])};
            PIN(add[mt]);
        }
    }, [&](int) {});
#pragma unroll
    for (int mt = 0; mt < 5; ++mt)
        st8(rout, (64 * rb + 16 * w + n) * 1280u + (80 * ct + 16 * mt + 4 * q) * 2u, (u32x2){pk(v[mt][0], v[mt][1]), pk(v[mt][2], v[mt][3])});

}

__global__ void __launch_bounds__(256, 1) k_all(Params p) {
    __shared__ __attribute__((aligned(16))) char smem[163840];
    char* scr = smem + LDS_SCR;

    unsigned pf_code;
    {
        u64 pc; unsigned len;
        asm volatile("s_getpc_b64 %0\nkall_pf_begin:\n\ts_mov_b32 %1, kall_pf_end-kall_pf_begin" : "=s"(pc), "=s"(len));
        pf_code = ld4(mkrs((const void*)pc, len), (blockIdx.x >> 3) * 4096u + threadIdx.x * 16u);
    }
#define PFL 3
    unsigned pf_line = 0;
    if (PFL) {
        const unsigned t = threadIdx.x, b = blockIdx.x, r = t / 9;
        const Map m0 = get_map(b);
        const char* a = p.ws + O_EPO;
        if ((PFL & 1) && t < 9 * NREP) a = p.ws + O_FLG + r * FREP + ((t - 9 * r) * 256 + b) * FSTR;
        if ((PFL & 2) && t == 255) a = p.ws + O_ST2 + b * 128u;
        if ((PFL & 2) && t == 254) a = p.ws + O_ST0 + (b & 63) * 128u;
        if ((PFL & 4) && t == 253) a = (const char*)p.b1 + m0.m * 192;
        if ((PFL & 4) && t == 252) a = (const char*)p.b1 + m0.m * 192 + 128;
        if ((PFL & 4) && t == 251) a = (const char*)p.b2 + m0.ct * 160;
        if ((PFL & 4) && t == 250) a = (const char*)p.b2 + m0.ct * 160 + 128;
        if ((PFL & 8) && t >= 192 && t < 208) a = (const char*)p.out + (64 * m0.rb + 4 * (t - 192)) * 1280 + m0.ct * 160;
        pf_line = __hip_atomic_load((const GAS unsigned*)a, __ATOMIC_RELAXED, __HIP_MEMORY_SCOPE_AGENT);
    }
#define MAP const int id = fresh_s(blockIdx.x); const Map mp = get_map(id)
    GAS u64* epw = (GAS u64*)(p.ws + O_EPO);
    const u64 ep_in = __hip_atomic_load(epw, __ATOMIC_RELAXED, __HIP_MEMORY_SCOPE_AGENT);

    u64 ep, fa;
    {
        MAP;

        if (mp.isC) {
            phase_p0(p, mp.g, mp.rb, mp.ct, smem);
            ep = rfl64(ep_in) + 1;
            phase_c<0>(p, mp.g, mp.tt, mp.m, mp.rb, mp.ct, ep, fa, smem, scr, [&]() { publish(p.ws, PH_P0, mp.g, ep); });
        } else {
            phase_aux(p, mp.ar, mp.tt, mp.m, mp.rb, mp.ct, ep_in, ep, fa, smem);
        }
        PIN(pf_code); PIN(pf_line);
    }

    u32x4 W[5][12], QT[5][5], xg[2][5][2];
    {
        MAP; IDS;
        const unsigned wo = O_W2F + ((5 * mp.ct * 48 + 12 * w) * 64 + lane) * 16u;
        phase_g1<0>(p, -1, mp.tt, mp.m, mp.rb, mp.ct, W, wo, xg, QT, ep, fa, smem);
        phase_g2(p, mp.rb, mp.ct, W, wo, ep, scr);
    }

    {
        MAP;
        const int i2 = mp.rb * 8 + mp.ct;
        if (mp.isC) phase_c<1>(p, mp.g, mp.tt, mp.m, mp.rb, mp.ct, ep, fa, smem, scr, [&]() { if (!PZ2) publish(p.ws, PH_G2, i2, ep); });
        else if (!PZ2) { publish(p.ws, PH_G2, i2, ep); }
        else if ((PH2 & 2) && MKH) {
            IDS;
#pragma unroll
            for (int i = 0; i < 6; ++i) mark_h2<96>(mkrs(p.ws, WS_USED), O_HF2, mp.ar, w, lane, i);
            publish(p.ws, PH_G2, mp.ar, ep);
        }
        else if (PZH) {
            IDS;
            mark_h(mkrs(p.ws, WS_USED), O_HF2, mp.tt, mp.m, w, lane);
            publish(p.ws, PH_G2, mp.ar, ep);
        }
    }
    {
        MAP; IDS;

        const unsigned wo = O_W2T + ((5 * mp.ct * 48 + 12 * w) * 64 + lane) * 16u;
        phase_g1<1>(p, mp.isC ? mp.g : -1, mp.tt, mp.m, mp.rb, mp.ct, W, wo, xg, QT, ep, fa, smem);
        phase_g4(p, mp.tt * 16 + mp.m, mp.rb, mp.ct, W, wo, QT, xg, ep, scr);
    }
    if (blockIdx.x == 0 && threadIdx.x == 0) __hip_atomic_store(epw, ep, __ATOMIC_RELAXED, __HIP_MEMORY_SCOPE_AGENT);

    asm volatile("kall_pf_end:");
}

extern "C" void kernel_launch(void* const* d_in, const int* in_sizes, int n_in, void* d_out, int out_size, void* d_ws, size_t ws_size,
                              hipStream_t stream) {
    Params p;
    p.x = (const uint16_t*)d_in[0]; p.wr = (const uint16_t*)d_in[1]; p.wi = (const uint16_t*)d_in[2];
    p.w1 = (const uint16_t*)d_in[4]; p.b1 = (const uint16_t*)d_in[5]; p.w2 = (const uint16_t*)d_in[6]; p.b2 = (const uint16_t*)d_in[7];
    p.out = (uint16_t*)d_out; p.ws = (char*)d_ws;
    k_all<<<256, 256, 0, stream>>>(p);
}
```
